```python
import torch

def _softmax(x, axis=-1):
    return torch.softmax(x, dim=axis)
def _gelu(x):
    return torch.nn.functional.gelu(x, approximate="tanh")
def _silu(x):
    return torch.nn.functional.silu(x)
def _sigmoid(x):
    return torch.sigmoid(x)
def _layernorm(x, axis=-1, eps=1e-5):
    m = x.mean(dim=axis, keepdim=True)
    v = x.var(dim=axis, keepdim=True, unbiased=False)
    return (x - m) / torch.sqrt(v + eps)
def _scatter_add(tgt, idx, src, axis=0):
    return tgt.clone().index_add_(axis, idx, src)
def _segment_sum(src, seg, n):
    out = torch.zeros((n,) + src.shape[1:], dtype=src.dtype)
    return out.index_add_(0, seg, src)
def _pad(x, pw):
    flat = tuple(v for pair in reversed(pw) for v in pair)
    return torch.nn.functional.pad(x, flat)

kernel_name = 'concat_heads__softmax_full_mha__grouped_kv_mqa__prompt'

def setup_inputs():

    _w = (lambda a: (lambda t: t.to(torch.bfloat16) if t.is_floating_point() else t)(torch.as_tensor(a)))
    def _A_setup():
        B, S, D = 1, 2048, 640
        return {
            "x": _w((lambda *s: torch.randn(*s, dtype=torch.bfloat16) * 0.2)(B, S, D)),
            "wqkv": _w((lambda *s: torch.randn(*s, dtype=torch.bfloat16) * 0.2)(D, 3 * D)),
            "wo": _w((lambda *s: torch.randn(*s, dtype=torch.bfloat16) * 0.05)(D, D)),
        }
    _A_in = _A_setup()

    def _B_setup():
        B, S, D, H = 1, 2048, 640, 32
        Dh = D // H
        return {
            "x": _w((lambda *s: torch.randn(*s, dtype=torch.bfloat16) * 0.05)(B, S, D)),
            "wq": _w((lambda *s: torch.randn(*s, dtype=torch.bfloat16) * 0.8)(D, H * Dh)),
            "wk": _w((lambda *s: torch.randn(*s, dtype=torch.bfloat16) * 0.8)(D, Dh)),
            "wv": _w((lambda *s: torch.randn(*s, dtype=torch.bfloat16) * 0.1)(D, Dh)),
            "wo": _w((lambda *s: torch.randn(*s, dtype=torch.bfloat16) * 0.1)(H * Dh, D)),
        }
    _B_in = _B_setup()

    return {
        "A_x": _A_in["x"],
        "A_wqkv": _A_in["wqkv"],
        "A_wo": _A_in["wo"],
        "B_x": _B_in["x"],
        "B_wq": _B_in["wq"],
        "B_wk": _B_in["wk"],
        "B_wv": _B_in["wv"],
        "B_wo": _B_in["wo"],
    }

def reference(A_x, A_wqkv, A_wo, B_x, B_wq, B_wk, B_wv, B_wo):
    def _A_ref(x, wqkv, wo):
        B, S, D = x.shape
        H = 32; Dh = D // H
        qkv = (lambda x, *d: x.permute(*d))((x @ wqkv).reshape(B, S, 3, H, Dh), 2, 0, 3, 1, 4)
        q, k, v = qkv[0], qkv[1], qkv[2]
        scale = Dh ** -0.5
        scores = torch.einsum("bhqd,bhkd->bhqk", q, k) * scale
        p = _softmax(scores, axis=-1)
        o = torch.einsum("bhqk,bhkd->bhqd", p, v)
        return (lambda x, *d: x.permute(*d))(o, 0, 2, 1, 3).reshape(B, S, D) @ wo

    def _B_ref(x, wq, wk, wv, wo):
        B, S, D = x.shape
        H = 32
        Dh = D // H
        q = (lambda x, *d: x.permute(*d))((x @ wq).reshape(B, S, H, Dh), 0, 2, 1, 3)
        k = x @ wk
        v = x @ wv
        scores = torch.einsum("bhqd,bkd->bhqk", q, k) * (Dh ** -0.5)
        p = _softmax(scores, axis=-1)
        o = torch.einsum("bhqk,bkd->bhqd", p, v)
        return (lambda x, *d: x.permute(*d))(o, 0, 2, 1, 3).reshape(B, S, H * Dh) @ wo

    a = _A_ref(A_x, A_wqkv, A_wo)
    b = _B_ref(B_x, B_wq, B_wk, B_wv, B_wo)
    return torch.concatenate([a, b], axis=-1)
```

```cpp
#include <hip/hip_runtime.h>
#include <hip/hip_bf16.h>
#include <stdint.h>

typedef short bf16x8 __attribute__((ext_vector_type(8)));
typedef short bf16x4 __attribute__((ext_vector_type(4)));
typedef float f32x16 __attribute__((ext_vector_type(16)));
typedef float f32x4 __attribute__((ext_vector_type(4)));
typedef float f32x2 __attribute__((ext_vector_type(2)));
typedef __bf16 bf16x2_t __attribute__((ext_vector_type(2)));
typedef unsigned u32x4 __attribute__((ext_vector_type(4)));
typedef unsigned u32x2 __attribute__((ext_vector_type(2)));
#define DI __device__ __forceinline__
#define AS3 __attribute__((address_space(3)))
typedef AS3 const char* lds_cptr;
typedef unsigned short u16;

constexpr int NH = 32;
constexpr int QTILE_B = 1280;
constexpr int KVTILE_B = 3072;
constexpr size_t Q_OFF = 0;
constexpr size_t Q_BYTES = (size_t)2 * NH * 64 * QTILE_B;
constexpr size_t KV_OFF = Q_OFF + Q_BYTES;
constexpr size_t KV_STREAM_B = (size_t)64 * KVTILE_B;
constexpr size_t KV_BYTES = 33 * KV_STREAM_B;
constexpr size_t O_OFF = KV_OFF + KV_BYTES;
constexpr size_t O_BYTES = (size_t)2 * 128 * 20 * 1024;
constexpr size_t WO_OFF = O_OFF + O_BYTES;
constexpr size_t WO_BYTES = (size_t)2 * 40 * 20 * 1024;
constexpr float QSCALE = 0.22360679774997896f * 1.4426950408889634f;

DI unsigned cvtpk(float lo, float hi) {
  f32x2 v = {lo, hi}; bf16x2_t b = __builtin_convertvector(v, bf16x2_t); return __builtin_bit_cast(unsigned, b);
}
#define MFMA32(a, b, c) __builtin_amdgcn_mfma_f32_32x32x16_bf16((a), (b), (c), 0, 0, 0)
#define MFMA32K8(a, b, c) __builtin_amdgcn_mfma_f32_32x32x8bf16_1k((a), (b), (c), 0, 0, 0)
#define MFMA16(a, b, c) __builtin_amdgcn_mfma_f32_16x16x32_bf16((a), (b), (c), 0, 0, 0)
#define SBAR() __builtin_amdgcn_sched_barrier(0)
#define WAITV(n) asm volatile("s_waitcnt vmcnt(" #n ")" ::: "memory")
#define WAITVL(n) asm volatile("s_waitcnt vmcnt(" #n ") lgkmcnt(0)" ::: "memory")
#define BARRIER() __builtin_amdgcn_s_barrier()
DI bf16x8 ldsr16(lds_cptr p) { return *(const AS3 bf16x8*)p; }
DI bf16x4 ldsr8(lds_cptr p) { return *(const AS3 bf16x4*)p; }
typedef short v4i16_t __attribute__((ext_vector_type(4)));
DI bf16x4 ldstr(lds_cptr p) { return __builtin_bit_cast(bf16x4, __builtin_amdgcn_ds_read_tr16_b64_v4i16((AS3 v4i16_t*)p)); }
DI bf16x8 cat8(bf16x4 a, bf16x4 b) { return __builtin_shufflevector(a, b, 0, 1, 2, 3, 4, 5, 6, 7); }

#define TS(i) do {} while (0)
#define TDECL do {} while (0)
#define TDUMP(kid, wsp, gw) do {} while (0)
DI void touch_lines(const char* sbase, unsigned voff, unsigned lds_dummy) {
  unsigned sv;
  asm volatile("s_mov_b32 %0, m0\n\ts_mov_b32 m0, %2\n\ts_nop 0\n\tglobal_load_lds_dword %3, %1\n\ts_mov_b32 m0, %0" : "=&s"(sv) : "s"(sbase), "s"(lds_dummy), "v"(voff) : "memory");
}
extern "C" __global__ void k1_proj(const char*, const char*, const char*, const char*, const char*, const char*, const u16*, const u16*, char*, const char*);
extern "C" __global__ void k2_attn(char*);
extern "C" __global__ void k3_out(const char*, u16*);
constexpr int K1_XB = 16384, K1_WB = 20480, K1_SLOT = K1_XB + K1_WB;
constexpr int K1_KVB = 5120, K1_SLOTB = K1_SLOT + K1_KVB;
constexpr int K1_LDS_ALL = 4 * K1_SLOT + 256;

template <bool ISK>
DI void store_qk_tile(char* ws, int inst, int n0, int row0, f32x4 acc, int lane) {
  const int g = lane >> 4, row = row0 + (lane & 15);
  const int G = (n0 >> 2) + g, head = (G * 52429) >> 18, dg = G - head * 5;
  const int tile = row >> 5, r32 = row & 31;
  const float sc = ISK ? 1.f : QSCALE;
  u32x2 v = {cvtpk(acc[0] * sc, acc[1] * sc), cvtpk(acc[2] * sc, acc[3] * sc)};
  const unsigned base = ISK ? (unsigned)KV_OFF + (unsigned)(inst ? 32 : head) * (unsigned)KV_STREAM_B + (unsigned)tile * KVTILE_B
                            : (unsigned)Q_OFF + ((unsigned)(inst * NH + head) * 64 + tile) * QTILE_B;
  const unsigned off = dg < 4 ? (dg >> 1) * 512 + r32 * 16 + (dg & 1) * 8 : 1024 + r32 * 8;
  *(u32x2*)(ws + (base + off)) = v;
}
DI void store_v_tile(char* ws, int inst, int n, int row0, f32x4 acc, int lane) {
  const int g = lane >> 4;
  const int head = (n * 52429) >> 20, d = n - head * 20;
  const int key = row0 + 4 * g, tile = key >> 5, k32 = key & 31;
  const int s = k32 >> 4, jj = (k32 >> 3) & 1, h = (k32 >> 2) & 1;
  u32x2 v = {cvtpk(acc[0], acc[1]), cvtpk(acc[2], acc[3])};
  const unsigned base = (unsigned)KV_OFF + (unsigned)(inst ? 32 : head) * (unsigned)KV_STREAM_B + (unsigned)tile * KVTILE_B;
  *(u32x2*)(ws + (base + 1536 + s * 768 + h * 384 + d * 16 + jj * 8)) = v;
}
DI void dma4(const char* sbase, unsigned ldsaddr, unsigned v0, unsigned v1, unsigned v2, unsigned v3) {
  unsigned sv;
  asm volatile("s_mov_b32 %0, m0\n\ts_mov_b32 m0, %2\n\ts_nop 0\n\t"
               "global_load_lds_dwordx4 %3, %1\n\tglobal_load_lds_dwordx4 %4, %1 offset:1024\n\tglobal_load_lds_dwordx4 %5, %1 offset:2048\n\tglobal_load_lds_dwordx4 %6, %1 offset:3072\n\t"
               "s_mov_b32 m0, %0" : "=&s"(sv) : "s"(sbase), "s"(ldsaddr), "v"(v0), "v"(v1), "v"(v2), "v"(v3) : "memory");
}
DI void dma2(const char* sbase, unsigned ldsaddr, unsigned v0, unsigned v1) {
  unsigned sv;
  asm volatile("s_mov_b32 %0, m0\n\ts_mov_b32 m0, %2\n\ts_nop 0\n\t"
               "global_load_lds_dwordx4 %3, %1\n\tglobal_load_lds_dwordx4 %4, %1 offset:1024\n\t"
               "s_mov_b32 m0, %0" : "=&s"(sv) : "s"(sbase), "s"(ldsaddr), "v"(v0), "v"(v1) : "memory");
}

DI void dma1(const char* sbase, unsigned ldsaddr, unsigned v0) {
  unsigned sv;
  asm volatile("s_mov_b32 %0, m0\n\ts_mov_b32 m0, %2\n\ts_nop 0\n\tglobal_load_lds_dwordx4 %3, %1\n\ts_mov_b32 m0, %0" : "=&s"(sv) : "s"(sbase), "s"(ldsaddr), "v"(v0) : "memory");
}
template <int NS>
DI void k1_loader(char* lds, const char* x, const char* w, int ldw2, const char* wk, const char* wv, int L, int lane, char* dbgws, int st) {
  constexpr int SLOT = NS == 3 ? K1_SLOTB : K1_SLOT;
  const unsigned lds0 = (unsigned)(uintptr_t)lds;
  TDECL; TS(0);
  unsigned vx[4], vw[5];
#pragma unroll
  for (int i = 0; i < 4; ++i) { const int row = 32 * L + 8 * i + (lane >> 3); vx[i] = (unsigned)(row * 1280 + (((lane & 7) ^ ((row >> 1) & 7)) << 4) - i * 1024); }
#pragma unroll
  for (int i = 0; i < 5; ++i) { const int c = (5 * L + i) * 64 + lane, k = (c * 52429) >> 20, cc = c - k * 20;
    vw[i] = (unsigned)(k * ldw2 + ((cc ^ (((k >> 3) & 1) << 1)) << 4) - (i & 3) * 1024 + 4096); }
  const char* wsrc = w - 4096;
  const char* kvsrc = (L < 2 ? wk : wv) + (L & 1) * 1280;
  const unsigned ldx = lds0 + L * 4096, ldw_ = lds0 + K1_XB + L * 5120, ldkv = lds0 + K1_SLOT + L * 1280, vo16 = lane * 16;
#define K1_STAGE(sq_, slot_) do { const int s_ = (sq_) + st >= 10 ? (sq_) + st - 10 : (sq_) + st; const char* xs_ = x + s_ * 128; const char* ws_ = wsrc + (size_t)s_ * 64 * ldw2; const unsigned o_ = (slot_) * SLOT; \
    dma4(xs_, ldx + o_, vx[0], vx[1], vx[2], vx[3]); dma4(ws_, ldw_ + o_, vw[0], vw[1], vw[2], vw[3]); dma1(ws_, ldw_ + o_ + 4096, vw[4]); \
    if constexpr (NS == 3) { dma1(kvsrc + s_ * 2560, ldkv + o_, vo16); if (lane < 16) dma1(kvsrc + s_ * 2560 + 1024, ldkv + o_ + 1024, vo16); } } while (0)
  TS(1);
  K1_STAGE(0, 0);
  TS(2);
  K1_STAGE(1, 1);
  TS(3);
  if constexpr (NS == 4) WAITV(9); else WAITV(11);
  TS(4);
  TDUMP(3, dbgws, blockIdx.x * 8 + L);
  BARRIER();
  K1_STAGE(2, 2);
  if constexpr (NS == 4) K1_STAGE(3, 3);
  int slot = 0;
#pragma unroll 1
  for (int s = 0; s < 9; ++s) {
    if constexpr (NS == 4) { if (s < 7) WAITV(18); else if (s == 7) WAITV(9); else WAITV(0); }
    else { if (s < 8) WAITV(11); else WAITV(0); }
    BARRIER();
    if (s + NS < 10) K1_STAGE(s + NS, slot);
    slot = slot == NS - 1 ? 0 : slot + 1;
  }
#undef K1_STAGE
}

template <int MODE>
DI void k1_body(char* lds, char* ws, int R, int cg, int wid, int lane, const u16* woA, const u16* woB, int st) {
  constexpr int NS = MODE == 2 ? 3 : 4, SLOT = MODE == 2 ? K1_SLOTB : K1_SLOT;
  TDECL; TS(0);
  const int wr = wid >> 1, wc = wid & 1, r = lane & 15, g = lane >> 4;
  const lds_cptr L = (lds_cptr)lds;
  const int xb0 = (64 * wr + r) * 128 + ((g ^ (r >> 1)) << 4), xb1 = xb0 ^ 64;
  const int q = (lane & 15) >> 2, p4 = lane & 3;
  const int wbase = K1_XB + (8 * g + q) * 320 + p4 * 8 + wc * 160;
  const int wbA = wbase + ((wc ^ g) & 1) * 32 - (wc & 1) * 32;
  const int wbB = wbase + (((wc ^ 1) ^ g) & 1) * 32 - ((wc ^ 1) & 1) * 32;
  const int kvb = K1_SLOT + wc * 2560 + (8 * g + q) * 40 + p4 * 8;
  f32x4 acc[4][5];
#pragma unroll
  for (int a = 0; a < 4; ++a)
#pragma unroll
    for (int b = 0; b < 5; ++b) acc[a][b] = f32x4{0, 0, 0, 0};
  f32x4 ex0 = {0, 0, 0, 0}, ex1 = {0, 0, 0, 0};
  bf16x8 xf0[4], wf0[5], xf1[4], wf1[5], xe0, xe1, k00, k01, k10, k11;
#define K1_LOAD(xf, wf, xe, k0, k1, B, ks, s_) do { \
    _Pragma("unroll") for (int a = 0; a < 4; ++a) xf[a] = ldsr16((B) + ((ks) ? xb1 : xb0) + a * 2048); \
    _Pragma("unroll") for (int b = 0; b < 5; ++b) { const lds_cptr pa = (B) + ((b & 1) ? wbB : wbA) + b * 32 + (ks) * 10240; wf[b] = cat8(ldstr(pa), ldstr(pa + 1280)); } \
    if constexpr (MODE == 2) { xe = ldsr16((B) + ((ks) ? xb1 : xb0) + cg * 2048); const lds_cptr pk = (B) + kvb + (ks) * 32 * 40; \
      k0 = cat8(ldstr(pk), ldstr(pk + 160)); k1 = cat8(ldstr(pk + 32), ldstr(pk + 192)); } } while (0)
#define K1_MMA(xf, wf, xe, k0, k1) do { \
    _Pragma("unroll") for (int a = 0; a < 4; ++a) _Pragma("unroll") for (int b = 0; b < 5; ++b) acc[a][b] = MODE == 1 ? MFMA16(xf[a], wf[b], acc[a][b]) : MFMA16(wf[b], xf[a], acc[a][b]); \
    if constexpr (MODE == 2) { ex0 = MFMA16(k0, xe, ex0); ex1 = MFMA16(k1, xe, ex1); } } while (0)
#define K1_SCHED() do { _Pragma("unroll") for (int i_ = 0; i_ < (MODE == 2 ? 19 : 14); ++i_) { __builtin_amdgcn_sched_group_barrier(0x008, 1, 0); __builtin_amdgcn_sched_group_barrier(0x100, 1, 0); } \
    __builtin_amdgcn_sched_group_barrier(0x008, 8, 0); } while (0)
  {
    const int gw = blockIdx.x * 4 + wid;
    bf16x8 v[2]; int fo[2];
#pragma unroll
    for (int i = 0; i < 2; ++i) {
      const int f = min(gw + 1024 * i, 1599), inst = f >= 800, ff = f - 800 * inst, cgp = ff / 100, rem = ff - cgp * 100, ks = rem / 5, nt = rem - ks * 5;
      const u16* src = (inst ? woB : woA) + (size_t)(ks * 32 + g * 8) * 640 + cgp * 80 + nt * 16 + r;
#pragma unroll
      for (int e = 0; e < 8; ++e) v[i][e] = (short)src[e * 640];
      fo[i] = f;
    }
#pragma unroll
    for (int i = 0; i < 2; ++i) *(bf16x8*)(ws + WO_OFF + (size_t)fo[i] * 1024 + lane * 16) = v[i];
  }
  TS(1);
  BARRIER();
  TS(2);
  K1_LOAD(xf0, wf0, xe0, k00, k01, L, 0, 0);
  int slot = 0;
#pragma unroll 1
  for (int s = 0; s < 9; ++s) {
    const lds_cptr B = L + slot * SLOT;
    slot = slot == NS - 1 ? 0 : slot + 1;
    SBAR();
    K1_LOAD(xf1, wf1, xe1, k10, k11, B, 1, s);
    K1_MMA(xf0, wf0, xe0, k00, k01);
    K1_SCHED();
    SBAR();
    asm volatile("s_waitcnt lgkmcnt(0)" ::: "memory");
    BARRIER();
    const lds_cptr Bn = L + slot * SLOT;
    SBAR();
    K1_LOAD(xf0, wf0, xe0, k00, k01, Bn, 0, s + 1);
    K1_MMA(xf1, wf1, xe1, k10, k11);
    K1_SCHED();
    SBAR();
  }
  { const lds_cptr B = L + slot * SLOT;
    K1_LOAD(xf1, wf1, xe1, k10, k11, B, 1, 9);
    K1_MMA(xf0, wf0, xe0, k00, k01);
    K1_SCHED();
    SBAR();
    K1_MMA(xf1, wf1, xe1, k10, k11); }
  TS(3);
  const int row0 = R * 128 + 64 * wr;
  if constexpr (MODE == 1) {
    char* base = ws + KV_OFF + (size_t)((cg & 3) * 8 + 4 * wc) * KV_STREAM_B + (size_t)(row0 >> 5) * KVTILE_B + 1536;
    const unsigned lo = (g & 1) * 384 + (g >> 1) * 8;
#pragma unroll
    for (int b = 0; b < 5; ++b) {
      const int n = 16 * b + r, hl = (n * 52429) >> 20, d = n - hl * 20;
      const unsigned vo = lo + hl * (unsigned)KV_STREAM_B + d * 16;
#pragma unroll
      for (int a = 0; a < 4; ++a) { u32x2 v = {cvtpk(acc[a][b][0], acc[a][b][1]), cvtpk(acc[a][b][2], acc[a][b][3])};
        *(u32x2*)(base + vo + ((a >> 1) * KVTILE_B + (a & 1) * 768)) = v; }
    }
  } else {
    const bool isK = MODE == 0 && cg >= 4;
    const unsigned hstride = isK ? (unsigned)KV_STREAM_B : 64u * QTILE_B, tstride = isK ? KVTILE_B : QTILE_B;
    const float sc = isK ? 1.f : QSCALE;
    char* base = ws + (isK ? KV_OFF : Q_OFF + (MODE == 2 ? (size_t)NH * 64 * QTILE_B : 0)) + (size_t)((cg & 3) * 8 + 4 * wc) * hstride + (size_t)(row0 >> 5) * tstride;
#pragma unroll
    for (int b = 0; b < 5; ++b) {
      const int e = 4 * b + g, hl = (e * 13) >> 6, dg = e - 5 * hl;
      const unsigned rs = dg < 4 ? 16 : 8;
      const unsigned vo = hl * hstride + (dg < 4 ? (dg >> 1) * 512 + (dg & 1) * 8 : 1024) + r * rs;
#pragma unroll
      for (int a = 0; a < 4; ++a) { u32x2 v = {cvtpk(acc[a][b][0] * sc, acc[a][b][1] * sc), cvtpk(acc[a][b][2] * sc, acc[a][b][3] * sc)};
        *(u32x2*)(base + (vo + (a >> 1) * tstride + (a & 1) * 16 * rs)) = v; }
    }
  }
  const u32x4 ones = {0x3F803F80u, 0x3F803F80u, 0x3F803F80u, 0x3F803F80u};
  const u32x4 kone = {0x3F80u, 0u, 0x3F80u, 0u};
  if constexpr (MODE == 0) { if (cg >= 4) {
#pragma unroll
      for (int i = 0; i < 2; ++i) { const int t = wid * 64 + lane + 256 * i, head = (cg & 3) * 8 + (t >> 6), tile = R * 4 + ((t >> 4) & 3);
        *(u32x4*)(ws + (KV_OFF + (size_t)head * KV_STREAM_B + (size_t)tile * KVTILE_B + 1280 + (t & 15) * 16)) = kone; } } }
  if constexpr (MODE == 1) {
    const int t = wid * 64 + lane;
    if (t < 128) { const int head = (cg & 3) * 8 + (t >> 4), tile = R * 4 + ((t >> 2) & 3), c = t & 3;
      *(u32x4*)(ws + (KV_OFF + (size_t)head * KV_STREAM_B + (size_t)tile * KVTILE_B + 1536 + c * 384 + 320)) = ones; }
  }
  if constexpr (MODE == 2) {
    const int er = row0 + 16 * cg;
    if (wc == 0) { store_qk_tile<true>(ws, 1, 0, er, ex0, lane); if (g == 0) store_qk_tile<true>(ws, 1, 16, er, ex1, lane);
      if (lane < 8) *(u32x4*)(ws + (KV_OFF + 32 * KV_STREAM_B + (size_t)(er >> 5) * KVTILE_B + 1280 + ((er >> 4) & 1) * 128 + lane * 16)) = kone; }
    else {
      const int k32 = (er + r) & 31;
      char* vb = ws + KV_OFF + 32 * KV_STREAM_B + (size_t)(er >> 5) * KVTILE_B + 1536 + (k32 >> 4) * 768 + ((k32 >> 2) & 1) * 384 + ((k32 >> 3) & 1) * 8 + (k32 & 3) * 2;
      const unsigned p0 = cvtpk(ex0[0], ex0[1]), p1 = cvtpk(ex0[2], ex0[3]), p2 = cvtpk(ex1[0], ex1[1]), p3 = cvtpk(ex1[2], ex1[3]);
      *(u16*)(vb + (4 * g) * 16) = (u16)p0; *(u16*)(vb + (4 * g + 1) * 16) = (u16)(p0 >> 16); *(u16*)(vb + (4 * g + 2) * 16) = (u16)p1; *(u16*)(vb + (4 * g + 3) * 16) = (u16)(p1 >> 16);
      if (g == 0) { *(u16*)(vb + 16 * 16) = (u16)p2; *(u16*)(vb + 17 * 16) = (u16)(p2 >> 16); *(u16*)(vb + 18 * 16) = (u16)p3; *(u16*)(vb + 19 * 16) = (u16)(p3 >> 16); }
      if (lane < 2) *(u32x4*)(ws + (KV_OFF + 32 * KV_STREAM_B + (size_t)(er >> 5) * KVTILE_B + 1536 + ((er >> 4) & 1) * 768 + lane * 384 + 320)) = ones; }
  }
  TS(4);
  TDUMP(0, ws, blockIdx.x * 8 + wid);
}

extern "C" __global__ __launch_bounds__(512, 2) void k1_proj(const char* xA, const char* wqkv, const char* xB, const char* wq, const char* wk, const char* wv,
                                                   const u16* woA, const u16* woB, char* ws, const char* outp) {
  extern __shared__ __attribute__((aligned(16))) char lds[];
  const int lane = threadIdx.x & 63, wid = __builtin_amdgcn_readfirstlane(threadIdx.x >> 6);
  const int xcd = blockIdx.x & 7, j = blockIdx.x >> 3;
  const bool isA = xcd < 6;
  const int R = (isA ? (xcd / 3) * 8 : (xcd - 6) * 8) + (j & 7), cg = (isA ? (xcd % 3) * 4 : 0) + (j >> 3);
#define ROT_MODE 1
  const int st = ROT_MODE == 0 ? 0 : ROT_MODE == 1 ? (3 * j) % 10 : ROT_MODE == 2 ? ((j + (j >> 3)) & 1) * 5 : (((j + (j >> 3)) % 3) * 10) / 3;
  if (wid >= 4) {
    if (isA) k1_loader<4>(lds, xA + (size_t)R * 128 * 1280, wqkv + cg * 320, 3840, nullptr, nullptr, wid - 4, lane, ws, st);
    else k1_loader<3>(lds, xB + (size_t)R * 128 * 1280, wq + cg * 320, 1280, wk, wv, wid - 4, lane, ws, st);
  } else {
    const unsigned dummy = (unsigned)(uintptr_t)lds + K1_LDS_ALL - 256;
    if (wid == 0 && j < 4) {
      unsigned long long pc; unsigned off, len;
      asm volatile("s_getpc_b64 %0\n1:\n\ts_mov_b32 %1, 1b-k1_proj\n\ts_mov_b32 %2, k3_out-k1_proj" : "=s"(pc), "=s"(off), "=s"(len));
      const unsigned vo = j * 8192 + lane * 128; if (vo < len + 4096) touch_lines((const char*)(pc - off), vo, dummy);
    }
    if (isA) { if (cg < 8) k1_body<0>(lds, ws, R, cg, wid, lane, woA, woB, st); else k1_body<1>(lds, ws, R, cg, wid, lane, woA, woB, st); }
    else k1_body<2>(lds, ws, R, cg, wid, lane, woA, woB, st);
  }
}

constexpr int NSLOT = 6, CHUNK_B = 8 * KVTILE_B;
constexpr int ATT_LDS = NSLOT * CHUNK_B + 256;
DI void dma_tile(const char* gsrc, unsigned voff, unsigned lds_dst) {
  unsigned sv;
  asm volatile("s_mov_b32 %0, m0\n\ts_mov_b32 m0, %3\n\ts_nop 0\n\tglobal_load_lds_dwordx4 %2, %1\n\tglobal_load_lds_dwordx4 %2, %1 offset:1024\n\tglobal_load_lds_dwordx4 %2, %1 offset:2048\n\ts_mov_b32 m0, %0"
               : "=&s"(sv) : "s"(gsrc), "v"(voff), "s"(lds_dst) : "memory");
}
DI float ex2(float x) { return __builtin_amdgcn_exp2f(x); }
DI float swap_max(float m) { auto rr = __builtin_amdgcn_permlane32_swap(__float_as_uint(m), __float_as_uint(m), false, false); return fmaxf(__uint_as_float(rr[0]), __uint_as_float(rr[1])); }
DI float max16(const f32x16& s) { float a = fmaxf(s[0], s[1]);
#pragma unroll
  for (int i = 2; i < 16; ++i) a = fmaxf(a, s[i]); return a; }
DI f32x16 splat16(float v) { f32x16 r;
#pragma unroll
  for (int i = 0; i < 16; ++i) r[i] = v; return r; }
#define BC8(x) __builtin_bit_cast(bf16x8, x)

extern "C" __global__ __launch_bounds__(576, 3) void k2_attn(char* ws) {
  extern __shared__ __attribute__((aligned(16))) char lds[];
  const int tid = threadIdx.x, lane = tid & 63, r32 = lane & 31, hi = lane >> 5;
  const int wid = __builtin_amdgcn_readfirstlane(tid >> 6);
  const int xcd = blockIdx.x & 7, j = blockIdx.x >> 3;
  const int inst = j >> 4, head = 4 * xcd + ((j & 15) >> 2), qb = j & 3;
  TDECL; TS(0);
  const char* kvs = ws + KV_OFF + (size_t)(inst ? 32 : head) * KV_STREAM_B;
  const char* gsrc = kvs + (size_t)wid * KVTILE_B;
  const unsigned lds0 = (unsigned)(uintptr_t)lds, ldst = lds0 + wid * KVTILE_B, vo16 = lane * 16;
  if (wid == 8) {
    { unsigned long long pc; unsigned off, len;
      asm volatile("s_getpc_b64 %0\n1:\n\ts_mov_b32 %1, 1b-k2_attn\n\ts_mov_b32 %2, k3_out-k2_attn" : "=s"(pc), "=s"(off), "=s"(len));
      if (j < 2 && lane * 128u < len) touch_lines((const char*)(pc - off), lane * 128, lds0 + NSLOT * CHUNK_B); }
#define K2_CHUNK(c, slot_) do { _Pragma("unroll") for (int i_ = 0; i_ < 6; ++i_) dma4(kvs + (size_t)(c) * CHUNK_B + i_ * 4096, lds0 + (slot_) * CHUNK_B + i_ * 4096, vo16, vo16, vo16, vo16); } while (0)
    BARRIER();
    K2_CHUNK(1, 1); K2_CHUNK(2, 2); WAITV(24); K2_CHUNK(3, 3); WAITV(24); K2_CHUNK(4, 4); WAITV(24);
    BARRIER();
    K2_CHUNK(5, 5); WAITV(0);
    BARRIER();
    K2_CHUNK(6, 0); K2_CHUNK(7, 1); WAITV(0);
    BARRIER();
    return;
  }
  dma_tile(gsrc, vo16, ldst);
  const int qt0 = qb * 16 + wid * 2;
  const char* qsrc = ws + Q_OFF + ((size_t)(inst * NH + head) * 64 + qt0) * QTILE_B;
  bf16x8 QA1, QB1; bf16x4 QA2, QB2;
  { const unsigned vo8 = r32 * 8;
    asm volatile("global_load_dwordx4 %0, %4, %6\n\tglobal_load_dwordx2 %1, %5, %6 offset:1024\n\tglobal_load_dwordx4 %2, %4, %6 offset:1280\n\tglobal_load_dwordx2 %3, %5, %6 offset:2304"
                 : "=&v"(QA1), "=&v"(QA2), "=&v"(QB1), "=&v"(QB2) : "v"(vo16), "v"(vo8), "s"(qsrc) : "memory"); }
  const lds_cptr kp1 = (lds_cptr)lds + lane * 16;
  const lds_cptr kp2 = (lds_cptr)lds + 1024 + lane * 8;
  const lds_cptr vp = (lds_cptr)lds + 1536 + hi * 384 + r32 * 16;
  f32x16 OA = splat16(0.f), OB = splat16(0.f);
  u32x4 PA0, PA1, PB0, PB1 = {0, 0, 0, 0};
  bf16x8 V0 = {0, 0, 0, 0, 0, 0, 0, 0}, V1 = {0, 0, 0, 0, 0, 0, 0, 0};
  asm volatile("s_waitcnt vmcnt(0)" : "+v"(QA1), "+v"(QA2), "+v"(QB1), "+v"(QB2) :: "memory");
  if (hi) { QA2 = bf16x4{0, 0, 0, 0}; QB2 = bf16x4{0, 0, 0, 0}; }
  TS(1);
  BARRIER();
  bf16x8 K1f = ldsr16(kp1); bf16x4 K2f = ldsr8(kp2);
  const f32x16 z16 = splat16(0.f);
  { const f32x16 a = MFMA32K8(K2f, QA2, MFMA32(K1f, QA1, z16)), b = MFMA32K8(K2f, QB2, MFMA32(K1f, QB1, z16));
    const unsigned na = __float_as_uint(-swap_max(max16(a))), nb = __float_as_uint(-swap_max(max16(b)));
    const unsigned ua = (na + ((na >> 31) ? 0u : 0xFFFFu)) >> 16, ub = (nb + ((nb >> 31) ? 0u : 0xFFFFu)) >> 16;
    if (hi) { QA2 = __builtin_bit_cast(bf16x4, u32x2{ua, 0u}); QB2 = __builtin_bit_cast(bf16x4, u32x2{ub, 0u}); } }
  f32x16 SA0 = MFMA32K8(K2f, QA2, MFMA32(K1f, QA1, z16)), SB0 = MFMA32K8(K2f, QB2, MFMA32(K1f, QB1, z16)), SA1, SB1;
  K1f = ldsr16(kp1 + KVTILE_B); K2f = ldsr8(kp2 + KVTILE_B);
#define GRP(S, i, P, k) do { SBAR(); S[i] = ex2(S[i]); S[i + 1] = ex2(S[i + 1]); S[i + 2] = ex2(S[i + 2]); S[i + 3] = ex2(S[i + 3]); \
      P[k] = cvtpk(S[i], S[i + 1]); P[k + 1] = cvtpk(S[i + 2], S[i + 3]); SBAR(); } while (0)
#define LDV(dst, p) dst = ldsr16(p)
#define LDK2(dst, p) dst = ldsr8(p)
#define LDK1(dst, p) dst = ldsr16(p)
#define STEP(SA, SB, SAn, SBn, voff, k1n, k2n) do { \
      LDV(V0, voff);                     SAn = MFMA32(K1f, QA1, z16);   GRP(SA, 0, PA0, 0); \
                                         SAn = MFMA32K8(K2f, QA2, SAn); GRP(SA, 4, PA0, 2); \
                                         OB = MFMA32(V1, BC8(PB1), OB); GRP(SA, 8, PA1, 0); \
      LDV(V1, (voff) + 768);             OA = MFMA32(V0, BC8(PA0), OA); GRP(SA, 12, PA1, 2); \
                                         SBn = MFMA32(K1f, QB1, z16);   GRP(SB, 0, PB0, 0); \
      LDK1(K1f, k1n);                    SBn = MFMA32K8(K2f, QB2, SBn); GRP(SB, 4, PB0, 2); \
      LDK2(K2f, k2n);                    OA = MFMA32(V1, BC8(PA1), OA); GRP(SB, 8, PB1, 0); \
                                         OB = MFMA32(V0, BC8(PB0), OB); GRP(SB, 12, PB1, 2); \
    } while (0)
#define PRIO(n) __builtin_amdgcn_s_setprio(n)
  TS(2);
  int slot = 0;
#pragma unroll 1
  for (int c = 0; c < 8; ++c) {
    const int so = slot * CHUNK_B;
    const int nslot = slot == NSLOT - 1 ? 0 : slot + 1;
    const int sn = nslot * CHUNK_B;
    const lds_cptr k1c = kp1 + so, k2c = kp2 + so, vc = vp + so, k1x = kp1 + sn, k2x = kp2 + sn;
    PRIO(2);
    STEP(SA0, SB0, SA1, SB1, vc + 0 * KVTILE_B, k1c + 2 * KVTILE_B, k2c + 2 * KVTILE_B);
    STEP(SA1, SB1, SA0, SB0, vc + 1 * KVTILE_B, k1c + 3 * KVTILE_B, k2c + 3 * KVTILE_B);
    PRIO(1);
    STEP(SA0, SB0, SA1, SB1, vc + 2 * KVTILE_B, k1c + 4 * KVTILE_B, k2c + 4 * KVTILE_B);
    STEP(SA1, SB1, SA0, SB0, vc + 3 * KVTILE_B, k1c + 5 * KVTILE_B, k2c + 5 * KVTILE_B);
    PRIO(0);
    STEP(SA0, SB0, SA1, SB1, vc + 4 * KVTILE_B, k1c + 6 * KVTILE_B, k2c + 6 * KVTILE_B);
    STEP(SA1, SB1, SA0, SB0, vc + 5 * KVTILE_B, k1c + 7 * KVTILE_B, k2c + 7 * KVTILE_B);
    if (c == 0 || c == 3 || c == 5) BARRIER();
    SBAR();
    PRIO(3);
    STEP(SA0, SB0, SA1, SB1, vc + 6 * KVTILE_B, k1x, k2x);
    STEP(SA1, SB1, SA0, SB0, vc + 7 * KVTILE_B, k1x + KVTILE_B, k2x + KVTILE_B);
    slot = nslot;
  }
  OB = MFMA32(V1, BC8(PB1), OB);
  TS(3);
  float lA, lB;
  { auto rr = __builtin_amdgcn_permlane32_swap(__float_as_uint(OA[8]), __float_as_uint(OA[8]), false, false); lA = __uint_as_float(rr[1]);
    auto rs = __builtin_amdgcn_permlane32_swap(__float_as_uint(OB[8]), __float_as_uint(OB[8]), false, false); lB = __uint_as_float(rs[1]); }
  float chk = 0.f;
#pragma unroll
  for (int i = 0; i < 9; ++i) { chk = fmaf(OA[i], 0.f, chk); chk = fmaf(OB[i], 0.f, chk); }
  if (!hi) {
#pragma unroll
    for (int i = 9; i < 12; ++i) { chk = fmaf(OA[i], 0.f, chk); chk = fmaf(OB[i], 0.f, chk); } }
  const bool bad = !(chk == 0.f) || !(lA < 1.8e19f) || !(lB < 1.8e19f);
  if (__builtin_expect(__any(bad), 0)) {
    const f32x16 z = splat16(0.f);
    if (hi) { QA2 = bf16x4{0, 0, 0, 0}; QB2 = bf16x4{0, 0, 0, 0}; }
    float mA = -INFINITY, mB = -INFINITY;
    for (int t = 0; t < 64; ++t) {
      const bf16x8 k1 = *(const bf16x8*)(kvs + (size_t)t * KVTILE_B + lane * 16);
      const bf16x4 k2 = *(const bf16x4*)(kvs + (size_t)t * KVTILE_B + 1024 + r32 * 8);
      mA = fmaxf(mA, max16(MFMA32K8(k2, QA2, MFMA32(k1, QA1, z))));
      mB = fmaxf(mB, max16(MFMA32K8(k2, QB2, MFMA32(k1, QB1, z))));
    }
    const f32x16 negA = splat16(-swap_max(mA)), negB = splat16(-swap_max(mB));
    OA = z; OB = z;
    for (int t = 0; t < 64; ++t) {
      const char* tb = kvs + (size_t)t * KVTILE_B;
      const bf16x8 k1 = *(const bf16x8*)(tb + lane * 16);
      const bf16x4 k2 = *(const bf16x4*)(tb + 1024 + r32 * 8);
      const bf16x8 v0 = *(const bf16x8*)(tb + 1536 + hi * 384 + r32 * 16), v1 = *(const bf16x8*)(tb + 1536 + 768 + hi * 384 + r32 * 16);
      f32x16 a = MFMA32K8(k2, QA2, MFMA32(k1, QA1, negA)), b = MFMA32K8(k2, QB2, MFMA32(k1, QB1, negB));
      u32x4 pa0, pa1, pb0, pb1;
#pragma unroll
      for (int i = 0; i < 4; ++i) { pa0[i] = cvtpk(ex2(a[2 * i]), ex2(a[2 * i + 1])); pa1[i] = cvtpk(ex2(a[8 + 2 * i]), ex2(a[9 + 2 * i]));
                                    pb0[i] = cvtpk(ex2(b[2 * i]), ex2(b[2 * i + 1])); pb1[i] = cvtpk(ex2(b[8 + 2 * i]), ex2(b[9 + 2 * i])); }
      OA = MFMA32(v1, BC8(pa1), MFMA32(v0, BC8(pa0), OA)); OB = MFMA32(v1, BC8(pb1), MFMA32(v0, BC8(pb0), OB));
    }
    auto rr = __builtin_amdgcn_permlane32_swap(__float_as_uint(OA[8]), __float_as_uint(OA[8]), false, false); lA = __uint_as_float(rr[1]);
    auto rs = __builtin_amdgcn_permlane32_swap(__float_as_uint(OB[8]), __float_as_uint(OB[8]), false, false); lB = __uint_as_float(rs[1]);
  }
  char* obase = ws + O_OFF + (size_t)inst * 128 * 20 * 1024 + (size_t)qt0 * 2 * 20 * 1024;
  const unsigned rowoff = (r32 >> 4) * 20 * 1024 + (r32 & 15) * 16;
#pragma unroll
  for (int q = 0; q < 2; ++q) {
    const f32x16& O = q ? OB : OA;
    const float inv = __builtin_amdgcn_rcpf(q ? lB : lA);
#pragma unroll
    for (int u = 0; u < 3; ++u) {
      const unsigned G = 5 * head + 2 * u + hi;
      if (u < 2 || !hi) {
        u32x2 v = {cvtpk(O[4 * u] * inv, O[4 * u + 1] * inv), cvtpk(O[4 * u + 2] * inv, O[4 * u + 3] * inv)};
        *(u32x2*)(obase + (rowoff + q * 2 * 20 * 1024 + (G >> 3) * 1024 + ((G >> 1) & 3) * 256 + (G & 1) * 8)) = v;
      }
    }
  }
  TS(4);
  TDUMP(1, ws, blockIdx.x * 8 + wid);
}

constexpr int K3_AB = 32768, K3_BB = 20480, K3_SLOT = K3_AB + K3_BB;
constexpr int K3_LDS = 3 * K3_SLOT + 256;
extern "C" __global__ __launch_bounds__(512, 2) void k3_out(const char* ws, u16* out) {
  extern __shared__ __attribute__((aligned(16))) char lds[];
  const int tid = threadIdx.x, lane = tid & 63, wid = __builtin_amdgcn_readfirstlane(tid >> 6);
  const int xcd = blockIdx.x & 7, j = blockIdx.x >> 3;
  const int inst = xcd >> 2, R = (xcd & 3) * 4 + (j & 3), cg = j >> 2;
  const unsigned lds0 = (unsigned)(uintptr_t)lds, vo16 = lane * 16;
  TDECL; TS(0);
  if (wid >= 4) {
    const int L = wid - 4;
    if (L == 3 && j < 2) { unsigned long long pc; unsigned off; asm volatile("s_getpc_b64 %0\n1:\n\ts_mov_b32 %1, 1b-k3_out" : "=s"(pc), "=s"(off));
      if (lane < 24) touch_lines((const char*)(pc - off), lane * 128, lds0 + 3 * K3_SLOT); }
    const char* wsrc = ws + WO_OFF + (size_t)(inst * 8 + cg) * 100 * 1024 + L * 5120;
    const char* osrc = ws + O_OFF + (size_t)(inst * 128 + R * 8 + 2 * L) * 20 * 1024;
#define K3_LD(G, slot) do { const unsigned o_ = lds0 + (slot) * K3_SLOT; \
      dma4(osrc + (G) * 4096, o_ + L * 8192, vo16, vo16, vo16, vo16); dma4(osrc + 20480 + (G) * 4096, o_ + L * 8192 + 4096, vo16, vo16, vo16, vo16); \
      dma4(wsrc + (G) * 20480, o_ + K3_AB + L * 5120, vo16, vo16, vo16, vo16); dma1(wsrc + (G) * 20480 + 4096, o_ + K3_AB + L * 5120 + 4096, vo16); } while (0)
    K3_LD(0, 0); K3_LD(1, 1); WAITV(13); BARRIER();
    K3_LD(2, 2); WAITV(13); BARRIER();
    K3_LD(3, 0); WAITV(13); BARRIER();
    K3_LD(4, 1); WAITV(13); BARRIER();
    WAITV(0); BARRIER();
  } else {
    f32x4 acc[2][5];
#pragma unroll
    for (int a = 0; a < 2; ++a)
#pragma unroll
      for (int b = 0; b < 5; ++b) acc[a][b] = f32x4{0, 0, 0, 0};
    const lds_cptr L = (lds_cptr)lds + lane * 16;
    bf16x8 wa[5], wb[5], oa[2], ob[2];
#define K3_LDF(wf, of, slot, k) do { const lds_cptr B_ = L + (slot) * K3_SLOT; of[0] = ldsr16(B_ + wid * 8192 + (k) * 1024); of[1] = ldsr16(B_ + wid * 8192 + 4096 + (k) * 1024); \
      _Pragma("unroll") for (int b = 0; b < 5; ++b) wf[b] = ldsr16(B_ + K3_AB + ((k) * 5 + b) * 1024); } while (0)
#define K3_MM(wf, of) do { _Pragma("unroll") for (int b = 0; b < 5; ++b) { acc[0][b] = MFMA16(wf[b], of[0], acc[0][b]); acc[1][b] = MFMA16(wf[b], of[1], acc[1][b]); } } while (0)
#define K3_IL() do { _Pragma("unroll") for (int i_ = 0; i_ < 7; ++i_) { __builtin_amdgcn_sched_group_barrier(0x008, 1, 0); __builtin_amdgcn_sched_group_barrier(0x100, 1, 0); } __builtin_amdgcn_sched_group_barrier(0x008, 3, 0); } while (0)
#define K3_STAGE3(slot) do { K3_LDF(wb, ob, slot, 1); K3_MM(wa, oa); K3_IL(); SBAR(); K3_LDF(wa, oa, slot, 2); K3_MM(wb, ob); K3_IL(); SBAR(); K3_LDF(wb, ob, slot, 3); K3_MM(wa, oa); K3_IL(); SBAR(); } while (0)
#define K3_NEXT(nslot) do { asm volatile("s_waitcnt lgkmcnt(0)" ::: "memory"); BARRIER(); SBAR(); K3_LDF(wa, oa, nslot, 0); K3_MM(wb, ob); K3_IL(); SBAR(); } while (0)
    TS(1);
    BARRIER();
    TS(2);
    K3_LDF(wa, oa, 0, 0); SBAR();
    K3_STAGE3(0); K3_NEXT(1);
    K3_STAGE3(1); K3_NEXT(2);
    K3_STAGE3(2); K3_NEXT(0);
    K3_STAGE3(0); K3_NEXT(1);
    K3_STAGE3(1);
    K3_MM(wb, ob);
    TS(3);
    AS3 char* S = (AS3 char*)lds + 2 * K3_SLOT;
    const int r = lane & 15, g = lane >> 4;
#pragma unroll
    for (int a = 0; a < 2; ++a)
#pragma unroll
      for (int b = 0; b < 5; ++b)
        *(AS3 u32x2*)(S + (wid * 32 + a * 16 + r) * 160 + b * 32 + g * 8) = u32x2{cvtpk(acc[a][b][0], acc[a][b][1]), cvtpk(acc[a][b][2], acc[a][b][3])};
    asm volatile("s_waitcnt lgkmcnt(0)" ::: "memory");
  }
  BARRIER();
  { const lds_cptr S = (lds_cptr)lds + 2 * K3_SLOT;
    char* ob = (char*)out + (size_t)R * 128 * 2560 + inst * 1280 + cg * 160;
#pragma unroll
    for (int i = 0; i < 3; ++i) { const int c = tid + 512 * i; if (c < 1280) { const int row = (c * 6554) >> 16, ch = c - row * 10; *(u32x4*)(ob + (unsigned)(row * 2560 + ch * 16)) = *(const AS3 u32x4*)(S + c * 16); } }
  }
  TS(4);
  if (wid < 4) TDUMP(2, ws, blockIdx.x * 8 + wid);
}

extern "C" void kernel_launch(void* const* d_in, const int* in_sizes, int n_in, void* d_out, int out_size, void* d_ws, size_t ws_size, hipStream_t stream) {
  char* ws = (char*)d_ws;
  (void)hipFuncSetAttribute((const void*)k1_proj, hipFuncAttributeMaxDynamicSharedMemorySize, K1_LDS_ALL);
  (void)hipFuncSetAttribute((const void*)k2_attn, hipFuncAttributeMaxDynamicSharedMemorySize, ATT_LDS);
  (void)hipFuncSetAttribute((const void*)k3_out, hipFuncAttributeMaxDynamicSharedMemorySize, K3_LDS);
  k1_proj<<<256, 512, K1_LDS_ALL, stream>>>((const char*)d_in[0], (const char*)d_in[1], (const char*)d_in[3], (const char*)d_in[4], (const char*)d_in[5], (const char*)d_in[6],
                                            (const u16*)d_in[2], (const u16*)d_in[7], ws, (const char*)d_out);
  k2_attn<<<256, 576, ATT_LDS, stream>>>(ws);
  k3_out<<<256, 512, K3_LDS, stream>>>(ws, (u16*)d_out);
}
```

```cpp
#include <hip/hip_runtime.h>
#include <hip/hip_bf16.h>
#include <stdint.h>

typedef short bf16x8 __attribute__((ext_vector_type(8)));
typedef short bf16x4 __attribute__((ext_vector_type(4)));
typedef float f32x16 __attribute__((ext_vector_type(16)));
typedef float f32x4 __attribute__((ext_vector_type(4)));
typedef float f32x2 __attribute__((ext_vector_type(2)));
typedef __bf16 bf16x2_t __attribute__((ext_vector_type(2)));
typedef unsigned u32x4 __attribute__((ext_vector_type(4)));
typedef unsigned u32x2 __attribute__((ext_vector_type(2)));
#define DI __device__ __forceinline__
#define AS3 __attribute__((address_space(3)))
typedef AS3 const char* lds_cptr;
typedef unsigned short u16;

constexpr int NH = 32;
constexpr int QTILE_B = 1280;
constexpr int KVTILE_B = 3072;
constexpr size_t Q_OFF = 0;
constexpr size_t Q_BYTES = (size_t)2 * NH * 64 * QTILE_B;
constexpr size_t KV_OFF = Q_OFF + Q_BYTES;
constexpr size_t KV_STREAM_B = (size_t)64 * KVTILE_B;
constexpr size_t KV_BYTES = 33 * KV_STREAM_B;
constexpr size_t O_OFF = KV_OFF + KV_BYTES;
constexpr size_t O_BYTES = (size_t)2 * 128 * 20 * 1024;
constexpr size_t WO_OFF = O_OFF + O_BYTES;
constexpr size_t WO_BYTES = (size_t)2 * 40 * 20 * 1024;
constexpr float QSCALE = 0.22360679774997896f * 1.4426950408889634f;

DI unsigned cvtpk(float lo, float hi) {
  f32x2 v = {lo, hi}; bf16x2_t b = __builtin_convertvector(v, bf16x2_t); return __builtin_bit_cast(unsigned, b);
}
#define MFMA32(a, b, c) __builtin_amdgcn_mfma_f32_32x32x16_bf16((a), (b), (c), 0, 0, 0)
#define MFMA32K8(a, b, c) __builtin_amdgcn_mfma_f32_32x32x8bf16_1k((a), (b), (c), 0, 0, 0)
#define MFMA16(a, b, c) __builtin_amdgcn_mfma_f32_16x16x32_bf16((a), (b), (c), 0, 0, 0)
#define SBAR() __builtin_amdgcn_sched_barrier(0)
#define WAITV(n) asm volatile("s_waitcnt vmcnt(" #n ")" ::: "memory")
#define WAITVL(n) asm volatile("s_waitcnt vmcnt(" #n ") lgkmcnt(0)" ::: "memory")
#define BARRIER() __builtin_amdgcn_s_barrier()
DI bf16x8 ldsr16(lds_cptr p) { return *(const AS3 bf16x8*)p; }
DI bf16x4 ldsr8(lds_cptr p) { return *(const AS3 bf16x4*)p; }
typedef short v4i16_t __attribute__((ext_vector_type(4)));
DI bf16x4 ldstr(lds_cptr p) { return __builtin_bit_cast(bf16x4, __builtin_amdgcn_ds_read_tr16_b64_v4i16((AS3 v4i16_t*)p)); }
DI bf16x8 cat8(bf16x4 a, bf16x4 b) { return __builtin_shufflevector(a, b, 0, 1, 2, 3, 4, 5, 6, 7); }

#define TS(i) do {} while (0)
#define TDECL do {} while (0)
#define TDUMP(kid, wsp, gw) do {} while (0)
DI void touch_lines(const char* sbase, unsigned voff, unsigned lds_dummy) {
  unsigned sv;
  asm volatile("s_mov_b32 %0, m0\n\ts_mov_b32 m0, %2\n\ts_nop 0\n\tglobal_load_lds_dword %3, %1\n\ts_mov_b32 m0, %0" : "=&s"(sv) : "s"(sbase), "s"(lds_dummy), "v"(voff) : "memory");
}
extern "C" __global__ void k1_proj(const char*, const char*, const char*, const char*, const char*, const char*, const u16*, const u16*, char*, const char*);
extern "C" __global__ void k2_attn(char*, const u16*, const u16*);
extern "C" __global__ void k3_out(const char*, u16*);
#define STF 0
#define STF3 0
template <int F> DI void st8f(void* p, u32x2 v) {
  if constexpr (F == 0) *(u32x2*)p = v;
  else if constexpr (F == 1) __builtin_nontemporal_store(v, (u32x2*)p);
  else asm volatile("global_store_dwordx2 %0, %1, off sc1" :: "v"(p), "v"(v) : "memory");
}
template <int F> DI void st16f(void* p, u32x4 v) {
  if constexpr (F == 0) *(u32x4*)p = v;
  else if constexpr (F == 1) __builtin_nontemporal_store(v, (u32x4*)p);
  else asm volatile("global_store_dwordx4 %0, %1, off sc1\n\ts_nop 1" :: "v"(p), "v"(v) : "memory");
}
constexpr int K1_XB = 16384, K1_WB = 20480, K1_SLOT = K1_XB + K1_WB;
constexpr int K1_KVB = 5120, K1_SLOTB = K1_SLOT + K1_KVB;
constexpr int K1_LDS_ALL = 4 * K1_SLOT + 256;

template <bool ISK>
DI void store_qk_tile(char* ws, int inst, int n0, int row0, f32x4 acc, int lane) {
  const int g = lane >> 4, row = row0 + (lane & 15);
  const int G = (n0 >> 2) + g, head = (G * 52429) >> 18, dg = G - head * 5;
  const int tile = row >> 5, r32 = row & 31;
  const float sc = ISK ? 1.f : QSCALE;
  u32x2 v = {cvtpk(acc[0] * sc, acc[1] * sc), cvtpk(acc[2] * sc, acc[3] * sc)};
  const unsigned base = ISK ? (unsigned)KV_OFF + (unsigned)(inst ? 32 : head) * (unsigned)KV_STREAM_B + (unsigned)tile * KVTILE_B
                            : (unsigned)Q_OFF + ((unsigned)(inst * NH + head) * 64 + tile) * QTILE_B;
  const unsigned off = dg < 4 ? (dg >> 1) * 512 + r32 * 16 + (dg & 1) * 8 : 1024 + r32 * 8;
  *(u32x2*)(ws + (base + off)) = v;
}
DI void store_v_tile(char* ws, int inst, int n, int row0, f32x4 acc, int lane) {
  const int g = lane >> 4;
  const int head = (n * 52429) >> 20, d = n - head * 20;
  const int key = row0 + 4 * g, tile = key >> 5, k32 = key & 31;
  const int s = k32 >> 4, jj = (k32 >> 3) & 1, h = (k32 >> 2) & 1;
  u32x2 v = {cvtpk(acc[0], acc[1]), cvtpk(acc[2], acc[3])};
  const unsigned base = (unsigned)KV_OFF + (unsigned)(inst ? 32 : head) * (unsigned)KV_STREAM_B + (unsigned)tile * KVTILE_B;
  *(u32x2*)(ws + (base + 1536 + s * 768 + h * 384 + d * 16 + jj * 8)) = v;
}
DI void dma4(const char* sbase, unsigned ldsaddr, unsigned v0, unsigned v1, unsigned v2, unsigned v3) {
  unsigned sv;
  asm volatile("s_mov_b32 %0, m0\n\ts_mov_b32 m0, %2\n\ts_nop 0\n\t"
               "global_load_lds_dwordx4 %3, %1\n\tglobal_load_lds_dwordx4 %4, %1 offset:1024\n\tglobal_load_lds_dwordx4 %5, %1 offset:2048\n\tglobal_load_lds_dwordx4 %6, %1 offset:3072\n\t"
               "s_mov_b32 m0, %0" : "=&s"(sv) : "s"(sbase), "s"(ldsaddr), "v"(v0), "v"(v1), "v"(v2), "v"(v3) : "memory");
}
DI void dma2(const char* sbase, unsigned ldsaddr, unsigned v0, unsigned v1) {
  unsigned sv;
  asm volatile("s_mov_b32 %0, m0\n\ts_mov_b32 m0, %2\n\ts_nop 0\n\t"
               "global_load_lds_dwordx4 %3, %1\n\tglobal_load_lds_dwordx4 %4, %1 offset:1024\n\t"
               "s_mov_b32 m0, %0" : "=&s"(sv) : "s"(sbase), "s"(ldsaddr), "v"(v0), "v"(v1) : "memory");
}

DI void dma1(const char* sbase, unsigned ldsaddr, unsigned v0) {
  unsigned sv;
  asm volatile("s_mov_b32 %0, m0\n\ts_mov_b32 m0, %2\n\ts_nop 0\n\tglobal_load_lds_dwordx4 %3, %1\n\ts_mov_b32 m0, %0" : "=&s"(sv) : "s"(sbase), "s"(ldsaddr), "v"(v0) : "memory");
}
template <int NS>
DI void k1_loader(char* lds, const char* x, const char* w, int ldw2, const char* wk, const char* wv, int L, int lane, char* dbgws, int st) {
  constexpr int SLOT = NS == 3 ? K1_SLOTB : K1_SLOT;
  const unsigned lds0 = (unsigned)(uintptr_t)lds;
  TDECL; TS(0);
  unsigned vx[4], vw[5];
#pragma unroll
  for (int i = 0; i < 4; ++i) { const int row = 32 * L + 8 * i + (lane >> 3); vx[i] = (unsigned)(row * 1280 + (((lane & 7) ^ ((row >> 1) & 7)) << 4) - i * 1024); }
#pragma unroll
  for (int i = 0; i < 5; ++i) { const int c = (5 * L + i) * 64 + lane, k = (c * 52429) >> 20, cc = c - k * 20;
    vw[i] = (unsigned)(k * ldw2 + ((cc ^ (((k >> 3) & 1) << 1)) << 4) - (i & 3) * 1024 + 4096); }
  const char* wsrc = w - 4096;
  const char* kvsrc = (L < 2 ? wk : wv) + (L & 1) * 1280;
  const unsigned ldx = lds0 + L * 4096, ldw_ = lds0 + K1_XB + L * 5120, ldkv = lds0 + K1_SLOT + L * 1280, vo16 = lane * 16;
#define K1_STAGE(sq_, slot_) do { const int s_ = (sq_) + st >= 10 ? (sq_) + st - 10 : (sq_) + st; const char* xs_ = x + s_ * 128; const char* ws_ = wsrc + (size_t)s_ * 64 * ldw2; const unsigned o_ = (slot_) * SLOT; \
    dma4(xs_, ldx + o_, vx[0], vx[1], vx[2], vx[3]); dma4(ws_, ldw_ + o_, vw[0], vw[1], vw[2], vw[3]); dma1(ws_, ldw_ + o_ + 4096, vw[4]); \
    if constexpr (NS == 3) { dma1(kvsrc + s_ * 2560, ldkv + o_, vo16); if (lane < 16) dma1(kvsrc + s_ * 2560 + 1024, ldkv + o_ + 1024, vo16); } } while (0)
  TS(1);
  K1_STAGE(0, 0);
  TS(2);
  K1_STAGE(1, 1);
  TS(3);
  if constexpr (NS == 4) WAITV(9); else WAITV(11);
  TS(4);
  TDUMP(3, dbgws, blockIdx.x * 8 + L);
  BARRIER();
  K1_STAGE(2, 2);
  if constexpr (NS == 4) K1_STAGE(3, 3);
  int slot = 0;
#pragma unroll 1
  for (int s = 0; s < 9; ++s) {
    if constexpr (NS == 4) { if (s < 7) WAITV(18); else if (s == 7) WAITV(9); else WAITV(0); }
    else { if (s < 8) WAITV(11); else WAITV(0); }
    BARRIER();
    if (s + NS < 10) K1_STAGE(s + NS, slot);
    slot = slot == NS - 1 ? 0 : slot + 1;
  }
#undef K1_STAGE
}

template <int MODE>
DI void k1_body(char* lds, char* ws, int R, int cg, int wid, int lane, const u16* woA, const u16* woB, int st) {
  constexpr int NS = MODE == 2 ? 3 : 4, SLOT = MODE == 2 ? K1_SLOTB : K1_SLOT;
  TDECL; TS(0);
  const int wr = wid >> 1, wc = wid & 1, r = lane & 15, g = lane >> 4;
  const lds_cptr L = (lds_cptr)lds;
  const int xb0 = (64 * wr + r) * 128 + ((g ^ (r >> 1)) << 4), xb1 = xb0 ^ 64;
  const int q = (lane & 15) >> 2, p4 = lane & 3;
  const int wbase = K1_XB + (8 * g + q) * 320 + p4 * 8 + wc * 160;
  const int wbA = wbase + ((wc ^ g) & 1) * 32 - (wc & 1) * 32;
  const int wbB = wbase + (((wc ^ 1) ^ g) & 1) * 32 - ((wc ^ 1) & 1) * 32;
  const int kvb = K1_SLOT + wc * 2560 + (8 * g + q) * 40 + p4 * 8;
  f32x4 acc[4][5];
#pragma unroll
  for (int a = 0; a < 4; ++a)
#pragma unroll
    for (int b = 0; b < 5; ++b) acc[a][b] = f32x4{0, 0, 0, 0};
  f32x4 ex0 = {0, 0, 0, 0}, ex1 = {0, 0, 0, 0};
  bf16x8 xf0[4], wf0[5], xf1[4], wf1[5], xe0, xe1, k00, k01, k10, k11;
#define K1_LOAD(xf, wf, xe, k0, k1, B, ks, s_) do { \
    _Pragma("unroll") for (int a = 0; a < 4; ++a) xf[a] = ldsr16((B) + ((ks) ? xb1 : xb0) + a * 2048); \
    _Pragma("unroll") for (int b = 0; b < 5; ++b) { const lds_cptr pa = (B) + ((b & 1) ? wbB : wbA) + b * 32 + (ks) * 10240; wf[b] = cat8(ldstr(pa), ldstr(pa + 1280)); } \
    if constexpr (MODE == 2) { xe = ldsr16((B) + ((ks) ? xb1 : xb0) + cg * 2048); const lds_cptr pk = (B) + kvb + (ks) * 32 * 40; \
      k0 = cat8(ldstr(pk), ldstr(pk + 160)); k1 = cat8(ldstr(pk + 32), ldstr(pk + 192)); } } while (0)
#define K1_MMA(xf, wf, xe, k0, k1) do { \
    _Pragma("unroll") for (int a = 0; a < 4; ++a) _Pragma("unroll") for (int b = 0; b < 5; ++b) acc[a][b] = MODE == 1 ? MFMA16(xf[a], wf[b], acc[a][b]) : MFMA16(wf[b], xf[a], acc[a][b]); \
    if constexpr (MODE == 2) { ex0 = MFMA16(k0, xe, ex0); ex1 = MFMA16(k1, xe, ex1); } } while (0)
#define K1_SCHED() do { _Pragma("unroll") for (int i_ = 0; i_ < (MODE == 2 ? 19 : 14); ++i_) { __builtin_amdgcn_sched_group_barrier(0x008, 1, 0); __builtin_amdgcn_sched_group_barrier(0x100, 1, 0); } \
    __builtin_amdgcn_sched_group_barrier(0x008, 8, 0); } while (0)
  TS(1);
  BARRIER();
  TS(2);
  K1_LOAD(xf0, wf0, xe0, k00, k01, L, 0, 0);
  int slot = 0;
#pragma unroll 1
  for (int s = 0; s < 9; ++s) {
    const lds_cptr B = L + slot * SLOT;
    slot = slot == NS - 1 ? 0 : slot + 1;
    SBAR();
    K1_LOAD(xf1, wf1, xe1, k10, k11, B, 1, s);
    K1_MMA(xf0, wf0, xe0, k00, k01);
    K1_SCHED();
    SBAR();
    asm volatile("s_waitcnt lgkmcnt(0)" ::: "memory");
    BARRIER();
    const lds_cptr Bn = L + slot * SLOT;
    SBAR();
    K1_LOAD(xf0, wf0, xe0, k00, k01, Bn, 0, s + 1);
    K1_MMA(xf1, wf1, xe1, k10, k11);
    K1_SCHED();
    SBAR();
  }
  { const lds_cptr B = L + slot * SLOT;
    K1_LOAD(xf1, wf1, xe1, k10, k11, B, 1, 9);
    K1_MMA(xf0, wf0, xe0, k00, k01);
    K1_SCHED();
    SBAR();
    K1_MMA(xf1, wf1, xe1, k10, k11); }
  TS(3);
  const int row0 = R * 128 + 64 * wr;
  if constexpr (MODE == 1) {
    char* base = ws + KV_OFF + (size_t)((cg & 3) * 8 + 4 * wc) * KV_STREAM_B + (size_t)(row0 >> 5) * KVTILE_B + 1536;
    const unsigned lo = (g & 1) * 384 + (g >> 1) * 8;
#pragma unroll
    for (int b = 0; b < 5; ++b) {
      const int n = 16 * b + r, hl = (n * 52429) >> 20, d = n - hl * 20;
      const unsigned vo = lo + hl * (unsigned)KV_STREAM_B + d * 16;
#pragma unroll
      for (int a = 0; a < 4; ++a) { u32x2 v = {cvtpk(acc[a][b][0], acc[a][b][1]), cvtpk(acc[a][b][2], acc[a][b][3])};
        st8f<STF>(base + vo + ((a >> 1) * KVTILE_B + (a & 1) * 768), v); }
    }
  } else {
    const bool isK = MODE == 0 && cg >= 4;
    const unsigned hstride = isK ? (unsigned)KV_STREAM_B : 64u * QTILE_B, tstride = isK ? KVTILE_B : QTILE_B;
    const float sc = isK ? 1.f : QSCALE;
    char* base = ws + (isK ? KV_OFF : Q_OFF + (MODE == 2 ? (size_t)NH * 64 * QTILE_B : 0)) + (size_t)((cg & 3) * 8 + 4 * wc) * hstride + (size_t)(row0 >> 5) * tstride;
#pragma unroll
    for (int b = 0; b < 5; ++b) {
      const int e = 4 * b + g, hl = (e * 13) >> 6, dg = e - 5 * hl;
      const unsigned rs = dg < 4 ? 16 : 8;
      const unsigned vo = hl * hstride + (dg < 4 ? (dg >> 1) * 512 + (dg & 1) * 8 : 1024) + r * rs;
#pragma unroll
      for (int a = 0; a < 4; ++a) { u32x2 v = {cvtpk(acc[a][b][0] * sc, acc[a][b][1] * sc), cvtpk(acc[a][b][2] * sc, acc[a][b][3] * sc)};
        st8f<STF>(base + (vo + (a >> 1) * tstride + (a & 1) * 16 * rs), v); }
    }
  }
  const u32x4 ones = {0x3F803F80u, 0x3F803F80u, 0x3F803F80u, 0x3F803F80u};
  const u32x4 kone = {0x3F80u, 0u, 0x3F80u, 0u};
  if constexpr (MODE == 0) { if (cg >= 4) {
#pragma unroll
      for (int i = 0; i < 2; ++i) { const int t = wid * 64 + lane + 256 * i, head = (cg & 3) * 8 + (t >> 6), tile = R * 4 + ((t >> 4) & 3);
        *(u32x4*)(ws + (KV_OFF + (size_t)head * KV_STREAM_B + (size_t)tile * KVTILE_B + 1280 + (t & 15) * 16)) = kone; } } }
  if constexpr (MODE == 1) {
    const int t = wid * 64 + lane;
    if (t < 128) { const int head = (cg & 3) * 8 + (t >> 4), tile = R * 4 + ((t >> 2) & 3), c = t & 3;
      *(u32x4*)(ws + (KV_OFF + (size_t)head * KV_STREAM_B + (size_t)tile * KVTILE_B + 1536 + c * 384 + 320)) = ones; }
  }
  if constexpr (MODE == 2) {
    const int er = row0 + 16 * cg;
    if (wc == 0) { store_qk_tile<true>(ws, 1, 0, er, ex0, lane); if (g == 0) store_qk_tile<true>(ws, 1, 16, er, ex1, lane);
      if (lane < 8) *(u32x4*)(ws + (KV_OFF + 32 * KV_STREAM_B + (size_t)(er >> 5) * KVTILE_B + 1280 + ((er >> 4) & 1) * 128 + lane * 16)) = kone; }
    else {
      const int k32 = (er + r) & 31;
      char* vb = ws + KV_OFF + 32 * KV_STREAM_B + (size_t)(er >> 5) * KVTILE_B + 1536 + (k32 >> 4) * 768 + ((k32 >> 2) & 1) * 384 + ((k32 >> 3) & 1) * 8 + (k32 & 3) * 2;
      const unsigned p0 = cvtpk(ex0[0], ex0[1]), p1 = cvtpk(ex0[2], ex0[3]), p2 = cvtpk(ex1[0], ex1[1]), p3 = cvtpk(ex1[2], ex1[3]);
      *(u16*)(vb + (4 * g) * 16) = (u16)p0; *(u16*)(vb + (4 * g + 1) * 16) = (u16)(p0 >> 16); *(u16*)(vb + (4 * g + 2) * 16) = (u16)p1; *(u16*)(vb + (4 * g + 3) * 16) = (u16)(p1 >> 16);
      if (g == 0) { *(u16*)(vb + 16 * 16) = (u16)p2; *(u16*)(vb + 17 * 16) = (u16)(p2 >> 16); *(u16*)(vb + 18 * 16) = (u16)p3; *(u16*)(vb + 19 * 16) = (u16)(p3 >> 16); }
      if (lane < 2) *(u32x4*)(ws + (KV_OFF + 32 * KV_STREAM_B + (size_t)(er >> 5) * KVTILE_B + 1536 + ((er >> 4) & 1) * 768 + lane * 384 + 320)) = ones; }
  }
  TS(4);
  TDUMP(0, ws, blockIdx.x * 8 + wid);
}

extern "C" __global__ __launch_bounds__(512, 2) void k1_proj(const char* xA, const char* wqkv, const char* xB, const char* wq, const char* wk, const char* wv,
                                                   const u16* woA, const u16* woB, char* ws, const char* outp) {
  extern __shared__ __attribute__((aligned(16))) char lds[];
  const int lane = threadIdx.x & 63, wid = __builtin_amdgcn_readfirstlane(threadIdx.x >> 6);
  const int xcd = blockIdx.x & 7, j = blockIdx.x >> 3;
  const bool isA = xcd < 6;
  const int R = (isA ? (xcd / 3) * 8 : (xcd - 6) * 8) + (j & 7), cg = (isA ? (xcd % 3) * 4 : 0) + (j >> 3);
#define ROT_MODE 1
  const int st = ROT_MODE == 0 ? 0 : ROT_MODE == 1 ? (3 * j) % 10 : ROT_MODE == 2 ? ((j + (j >> 3)) & 1) * 5 : (((j + (j >> 3)) % 3) * 10) / 3;
  if (wid < 4) {
    if (isA) k1_loader<4>(lds, xA + (size_t)R * 128 * 1280, wqkv + cg * 320, 3840, nullptr, nullptr, wid, lane, ws, st);
    else k1_loader<3>(lds, xB + (size_t)R * 128 * 1280, wq + cg * 320, 1280, wk, wv, wid, lane, ws, st);
  } else {
    const unsigned dummy = (unsigned)(uintptr_t)lds + K1_LDS_ALL - 256;
    if (wid == 4 && j < 4) {
      unsigned long long pc; unsigned off, len;
      asm volatile("s_getpc_b64 %0\n1:\n\ts_mov_b32 %1, 1b-k1_proj\n\ts_mov_b32 %2, k3_out-k1_proj" : "=s"(pc), "=s"(off), "=s"(len));
      const unsigned vo = j * 8192 + lane * 128; if (vo < len + 4096) touch_lines((const char*)(pc - off), vo, dummy);
    }
    if (isA) { if (cg < 8) k1_body<0>(lds, ws, R, cg, wid - 4, lane, woA, woB, st); else k1_body<1>(lds, ws, R, cg, wid - 4, lane, woA, woB, st); }
    else k1_body<2>(lds, ws, R, cg, wid - 4, lane, woA, woB, st);
  }
}

constexpr int NSLOT = 6, CHUNK_B = 8 * KVTILE_B;
constexpr int ATT_LDS = NSLOT * CHUNK_B + 256;
DI void dma_tile(const char* gsrc, unsigned voff, unsigned lds_dst) {
  unsigned sv;
  asm volatile("s_mov_b32 %0, m0\n\ts_mov_b32 m0, %3\n\ts_nop 0\n\tglobal_load_lds_dwordx4 %2, %1\n\tglobal_load_lds_dwordx4 %2, %1 offset:1024\n\tglobal_load_lds_dwordx4 %2, %1 offset:2048\n\ts_mov_b32 m0, %0"
               : "=&s"(sv) : "s"(gsrc), "v"(voff), "s"(lds_dst) : "memory");
}
DI float ex2(float x) { return __builtin_amdgcn_exp2f(x); }
DI float swap_max(float m) { auto rr = __builtin_amdgcn_permlane32_swap(__float_as_uint(m), __float_as_uint(m), false, false); return fmaxf(__uint_as_float(rr[0]), __uint_as_float(rr[1])); }
DI float max16(const f32x16& s) { float a = fmaxf(s[0], s[1]);
#pragma unroll
  for (int i = 2; i < 16; ++i) a = fmaxf(a, s[i]); return a; }
DI f32x16 splat16(float v) { f32x16 r;
#pragma unroll
  for (int i = 0; i < 16; ++i) r[i] = v; return r; }
#define BC8(x) __builtin_bit_cast(bf16x8, x)

extern "C" __global__ __launch_bounds__(576, 3) void k2_attn(char* ws, const u16* woA, const u16* woB) {
  extern __shared__ __attribute__((aligned(16))) char lds[];
  const int tid = threadIdx.x, lane = tid & 63, r32 = lane & 31, hi = lane >> 5;
  const int wid = __builtin_amdgcn_readfirstlane(tid >> 6);
  const int xcd = blockIdx.x & 7, j = blockIdx.x >> 3;
  const int inst = j >> 4, head = 4 * xcd + ((j & 15) >> 2), qb = j & 3;
  TDECL; TS(0);
  const char* kvs = ws + KV_OFF + (size_t)(inst ? 32 : head) * KV_STREAM_B;
  const char* gsrc = kvs + (size_t)wid * KVTILE_B;
  const unsigned lds0 = (unsigned)(uintptr_t)lds, ldst = lds0 + wid * KVTILE_B, vo16 = lane * 16;
  if (wid == 8) {
    { unsigned long long pc; unsigned off, len;
      asm volatile("s_getpc_b64 %0\n1:\n\ts_mov_b32 %1, 1b-k2_attn\n\ts_mov_b32 %2, k3_out-k2_attn" : "=s"(pc), "=s"(off), "=s"(len));
      if (j < 2 && lane * 128u < len) touch_lines((const char*)(pc - off), lane * 128, lds0 + NSLOT * CHUNK_B); }
#define K2_CHUNK(c, slot_) do { _Pragma("unroll") for (int i_ = 0; i_ < 6; ++i_) dma4(kvs + (size_t)(c) * CHUNK_B + i_ * 4096, lds0 + (slot_) * CHUNK_B + i_ * 4096, vo16, vo16, vo16, vo16); } while (0)
    BARRIER();
    K2_CHUNK(1, 1); K2_CHUNK(2, 2); WAITV(24); K2_CHUNK(3, 3); WAITV(24); K2_CHUNK(4, 4); WAITV(24);
    BARRIER();
    K2_CHUNK(5, 5); WAITV(0);
    BARRIER();
    K2_CHUNK(6, 0); K2_CHUNK(7, 1); WAITV(0);
    BARRIER();
    bf16x8 wv_[7];
#pragma unroll
    for (int i = 0; i < 7; ++i) {
      const int f = min((int)blockIdx.x + 256 * i, 1599), inst_ = f >= 800, ff = f - 800 * inst_, cgp = ff / 100, rem = ff - cgp * 100, ks = rem / 5, nt = rem - ks * 5;
      const u16* src = (inst_ ? woB : woA) + (size_t)(ks * 32 + (lane >> 4) * 8) * 640 + cgp * 80 + nt * 16 + (lane & 15);
#pragma unroll
      for (int e = 0; e < 8; ++e) wv_[i][e] = (short)src[e * 640];
    }
#pragma unroll
    for (int i = 0; i < 7; ++i) if ((int)blockIdx.x + 256 * i < 1600) *(bf16x8*)(ws + WO_OFF + (size_t)(blockIdx.x + 256 * i) * 1024 + lane * 16) = wv_[i];
    return;
  }
  dma_tile(gsrc, vo16, ldst);
  const int qt0 = qb * 16 + wid * 2;
  const char* qsrc = ws + Q_OFF + ((size_t)(inst * NH + head) * 64 + qt0) * QTILE_B;
  bf16x8 QA1, QB1; bf16x4 QA2, QB2;
  { const unsigned vo8 = r32 * 8;
    asm volatile("global_load_dwordx4 %0, %4, %6\n\tglobal_load_dwordx2 %1, %5, %6 offset:1024\n\tglobal_load_dwordx4 %2, %4, %6 offset:1280\n\tglobal_load_dwordx2 %3, %5, %6 offset:2304"
                 : "=&v"(QA1), "=&v"(QA2), "=&v"(QB1), "=&v"(QB2) : "v"(vo16), "v"(vo8), "s"(qsrc) : "memory"); }
  const lds_cptr kp1 = (lds_cptr)lds + lane * 16;
  const lds_cptr kp2 = (lds_cptr)lds + 1024 + lane * 8;
  const lds_cptr vp = (lds_cptr)lds + 1536 + hi * 384 + r32 * 16;
  f32x16 OA = splat16(0.f), OB = splat16(0.f);
  u32x4 PA0, PA1, PB0, PB1 = {0, 0, 0, 0};
  bf16x8 V0 = {0, 0, 0, 0, 0, 0, 0, 0}, V1 = {0, 0, 0, 0, 0, 0, 0, 0};
  asm volatile("s_waitcnt vmcnt(0)" : "+v"(QA1), "+v"(QA2), "+v"(QB1), "+v"(QB2) :: "memory");
  if (hi) { QA2 = bf16x4{0, 0, 0, 0}; QB2 = bf16x4{0, 0, 0, 0}; }
  TS(1);
  BARRIER();
  bf16x8 K1f = ldsr16(kp1); bf16x4 K2f = ldsr8(kp2);
  const f32x16 z16 = splat16(0.f);
  { const f32x16 a = MFMA32K8(K2f, QA2, MFMA32(K1f, QA1, z16)), b = MFMA32K8(K2f, QB2, MFMA32(K1f, QB1, z16));
    const unsigned na = __float_as_uint(-swap_max(max16(a))), nb = __float_as_uint(-swap_max(max16(b)));
    const unsigned ua = (na + ((na >> 31) ? 0u : 0xFFFFu)) >> 16, ub = (nb + ((nb >> 31) ? 0u : 0xFFFFu)) >> 16;
    if (hi) { QA2 = __builtin_bit_cast(bf16x4, u32x2{ua, 0u}); QB2 = __builtin_bit_cast(bf16x4, u32x2{ub, 0u}); } }
  f32x16 SA0 = MFMA32K8(K2f, QA2, MFMA32(K1f, QA1, z16)), SB0 = MFMA32K8(K2f, QB2, MFMA32(K1f, QB1, z16)), SA1, SB1;
  K1f = ldsr16(kp1 + KVTILE_B); K2f = ldsr8(kp2 + KVTILE_B);
#define GRP(S, i, P, k) do { SBAR(); S[i] = ex2(S[i]); S[i + 1] = ex2(S[i + 1]); S[i + 2] = ex2(S[i + 2]); S[i + 3] = ex2(S[i + 3]); \
      P[k] = cvtpk(S[i], S[i + 1]); P[k + 1] = cvtpk(S[i + 2], S[i + 3]); SBAR(); } while (0)
#define LDV(dst, p) dst = ldsr16(p)
#define LDK2(dst, p) dst = ldsr8(p)
#define LDK1(dst, p) dst = ldsr16(p)
#define STEP(SA, SB, SAn, SBn, voff, k1n, k2n) do { \
      LDV(V0, voff);                     SAn = MFMA32(K1f, QA1, z16);   GRP(SA, 0, PA0, 0); \
                                         SAn = MFMA32K8(K2f, QA2, SAn); GRP(SA, 4, PA0, 2); \
                                         OB = MFMA32(V1, BC8(PB1), OB); GRP(SA, 8, PA1, 0); \
      LDV(V1, (voff) + 768);             OA = MFMA32(V0, BC8(PA0), OA); GRP(SA, 12, PA1, 2); \
                                         SBn = MFMA32(K1f, QB1, z16);   GRP(SB, 0, PB0, 0); \
      LDK1(K1f, k1n);                    SBn = MFMA32K8(K2f, QB2, SBn); GRP(SB, 4, PB0, 2); \
      LDK2(K2f, k2n);                    OA = MFMA32(V1, BC8(PA1), OA); GRP(SB, 8, PB1, 0); \
                                         OB = MFMA32(V0, BC8(PB0), OB); GRP(SB, 12, PB1, 2); \
    } while (0)
#define PRIO(n) __builtin_amdgcn_s_setprio(n)
  TS(2);
  int slot = 0;
#pragma unroll 1
  for (int c = 0; c < 8; ++c) {
    const int so = slot * CHUNK_B;
    const int nslot = slot == NSLOT - 1 ? 0 : slot + 1;
    const int sn = nslot * CHUNK_B;
    const lds_cptr k1c = kp1 + so, k2c = kp2 + so, vc = vp + so, k1x = kp1 + sn, k2x = kp2 + sn;
    PRIO(2);
    STEP(SA0, SB0, SA1, SB1, vc + 0 * KVTILE_B, k1c + 2 * KVTILE_B, k2c + 2 * KVTILE_B);
    STEP(SA1, SB1, SA0, SB0, vc + 1 * KVTILE_B, k1c + 3 * KVTILE_B, k2c + 3 * KVTILE_B);
    PRIO(1);
    STEP(SA0, SB0, SA1, SB1, vc + 2 * KVTILE_B, k1c + 4 * KVTILE_B, k2c + 4 * KVTILE_B);
    STEP(SA1, SB1, SA0, SB0, vc + 3 * KVTILE_B, k1c + 5 * KVTILE_B, k2c + 5 * KVTILE_B);
    PRIO(0);
    STEP(SA0, SB0, SA1, SB1, vc + 4 * KVTILE_B, k1c + 6 * KVTILE_B, k2c + 6 * KVTILE_B);
    STEP(SA1, SB1, SA0, SB0, vc + 5 * KVTILE_B, k1c + 7 * KVTILE_B, k2c + 7 * KVTILE_B);
    if (c == 0 || c == 3 || c == 5) BARRIER();
    SBAR();
    PRIO(3);
    STEP(SA0, SB0, SA1, SB1, vc + 6 * KVTILE_B, k1x, k2x);
    STEP(SA1, SB1, SA0, SB0, vc + 7 * KVTILE_B, k1x + KVTILE_B, k2x + KVTILE_B);
    slot = nslot;
  }
  OB = MFMA32(V1, BC8(PB1), OB);
  TS(3);
  float lA, lB;
  { auto rr = __builtin_amdgcn_permlane32_swap(__float_as_uint(OA[8]), __float_as_uint(OA[8]), false, false); lA = __uint_as_float(rr[1]);
    auto rs = __builtin_amdgcn_permlane32_swap(__float_as_uint(OB[8]), __float_as_uint(OB[8]), false, false); lB = __uint_as_float(rs[1]); }
  float chk = 0.f;
#pragma unroll
  for (int i = 0; i < 9; ++i) { chk = fmaf(OA[i], 0.f, chk); chk = fmaf(OB[i], 0.f, chk); }
  if (!hi) {
#pragma unroll
    for (int i = 9; i < 12; ++i) { chk = fmaf(OA[i], 0.f, chk); chk = fmaf(OB[i], 0.f, chk); } }
  const bool bad = !(chk == 0.f) || !(lA < 1.8e19f) || !(lB < 1.8e19f);
  if (__builtin_expect(__any(bad), 0)) {
    const f32x16 z = splat16(0.f);
    if (hi) { QA2 = bf16x4{0, 0, 0, 0}; QB2 = bf16x4{0, 0, 0, 0}; }
    float mA = -INFINITY, mB = -INFINITY;
    for (int t = 0; t < 64; ++t) {
      const bf16x8 k1 = *(const bf16x8*)(kvs + (size_t)t * KVTILE_B + lane * 16);
      const bf16x4 k2 = *(const bf16x4*)(kvs + (size_t)t * KVTILE_B + 1024 + r32 * 8);
      mA = fmaxf(mA, max16(MFMA32K8(k2, QA2, MFMA32(k1, QA1, z))));
      mB = fmaxf(mB, max16(MFMA32K8(k2, QB2, MFMA32(k1, QB1, z))));
    }
    const f32x16 negA = splat16(-swap_max(mA)), negB = splat16(-swap_max(mB));
    OA = z; OB = z;
    for (int t = 0; t < 64; ++t) {
      const char* tb = kvs + (size_t)t * KVTILE_B;
      const bf16x8 k1 = *(const bf16x8*)(tb + lane * 16);
      const bf16x4 k2 = *(const bf16x4*)(tb + 1024 + r32 * 8);
      const bf16x8 v0 = *(const bf16x8*)(tb + 1536 + hi * 384 + r32 * 16), v1 = *(const bf16x8*)(tb + 1536 + 768 + hi * 384 + r32 * 16);
      f32x16 a = MFMA32K8(k2, QA2, MFMA32(k1, QA1, negA)), b = MFMA32K8(k2, QB2, MFMA32(k1, QB1, negB));
      u32x4 pa0, pa1, pb0, pb1;
#pragma unroll
      for (int i = 0; i < 4; ++i) { pa0[i] = cvtpk(ex2(a[2 * i]), ex2(a[2 * i + 1])); pa1[i] = cvtpk(ex2(a[8 + 2 * i]), ex2(a[9 + 2 * i]));
                                    pb0[i] = cvtpk(ex2(b[2 * i]), ex2(b[2 * i + 1])); pb1[i] = cvtpk(ex2(b[8 + 2 * i]), ex2(b[9 + 2 * i])); }
      OA = MFMA32(v1, BC8(pa1), MFMA32(v0, BC8(pa0), OA)); OB = MFMA32(v1, BC8(pb1), MFMA32(v0, BC8(pb0), OB));
    }
    auto rr = __builtin_amdgcn_permlane32_swap(__float_as_uint(OA[8]), __float_as_uint(OA[8]), false, false); lA = __uint_as_float(rr[1]);
    auto rs = __builtin_amdgcn_permlane32_swap(__float_as_uint(OB[8]), __float_as_uint(OB[8]), false, false); lB = __uint_as_float(rs[1]);
  }
  char* obase = ws + O_OFF + (size_t)inst * 128 * 20 * 1024 + (size_t)qt0 * 2 * 20 * 1024;
  const unsigned rowoff = (r32 >> 4) * 20 * 1024 + (r32 & 15) * 16;
#pragma unroll
  for (int q = 0; q < 2; ++q) {
    const f32x16& O = q ? OB : OA;
    const float inv = __builtin_amdgcn_rcpf(q ? lB : lA);
#pragma unroll
    for (int u = 0; u < 3; ++u) {
      const unsigned G = 5 * head + 2 * u + hi;
      if (u < 2 || !hi) {
        u32x2 v = {cvtpk(O[4 * u] * inv, O[4 * u + 1] * inv), cvtpk(O[4 * u + 2] * inv, O[4 * u + 3] * inv)};
        st8f<STF>(obase + (rowoff + q * 2 * 20 * 1024 + (G >> 3) * 1024 + ((G >> 1) & 3) * 256 + (G & 1) * 8), v);
      }
    }
  }
  TS(4);
  TDUMP(1, ws, blockIdx.x * 8 + wid);
}

constexpr int K3_AB = 32768, K3_BB = 20480, K3_SLOT = K3_AB + K3_BB;
constexpr int K3_LDS = 3 * K3_SLOT + 256;
extern "C" __global__ __launch_bounds__(512, 2) void k3_out(const char* ws, u16* out) {
  extern __shared__ __attribute__((aligned(16))) char lds[];
  const int tid = threadIdx.x, lane = tid & 63, wid = __builtin_amdgcn_readfirstlane(tid >> 6);
  const int xcd = blockIdx.x & 7, j = blockIdx.x >> 3;
  const int inst = xcd >> 2, R = (xcd & 3) * 4 + (j & 3), cg = j >> 2;
  const unsigned lds0 = (unsigned)(uintptr_t)lds, vo16 = lane * 16;
  TDECL; TS(0);
  if (wid < 4) {
    const int L = wid;
    if (L == 3 && j < 2) { unsigned long long pc; unsigned off; asm volatile("s_getpc_b64 %0\n1:\n\ts_mov_b32 %1, 1b-k3_out" : "=s"(pc), "=s"(off));
      if (lane < 24) touch_lines((const char*)(pc - off), lane * 128, lds0 + 3 * K3_SLOT); }
    const char* wsrc = ws + WO_OFF + (size_t)(inst * 8 + cg) * 100 * 1024 + L * 5120;
    const char* osrc = ws + O_OFF + (size_t)(inst * 128 + R * 8 + 2 * L) * 20 * 1024;
#define K3_LD(G, slot) do { const unsigned o_ = lds0 + (slot) * K3_SLOT; \
      dma4(osrc + (G) * 4096, o_ + L * 8192, vo16, vo16, vo16, vo16); dma4(osrc + 20480 + (G) * 4096, o_ + L * 8192 + 4096, vo16, vo16, vo16, vo16); \
      dma4(wsrc + (G) * 20480, o_ + K3_AB + L * 5120, vo16, vo16, vo16, vo16); dma1(wsrc + (G) * 20480 + 4096, o_ + K3_AB + L * 5120 + 4096, vo16); } while (0)
    K3_LD(0, 0); K3_LD(1, 1); WAITV(13); BARRIER();
    K3_LD(2, 2); WAITV(13); BARRIER();
    K3_LD(3, 0); WAITV(13); BARRIER();
    K3_LD(4, 1); WAITV(13); BARRIER();
    WAITV(0); BARRIER();
  } else {
    const int cw = wid - 4;
    f32x4 acc[2][5];
#pragma unroll
    for (int a = 0; a < 2; ++a)
#pragma unroll
      for (int b = 0; b < 5; ++b) acc[a][b] = f32x4{0, 0, 0, 0};
    const lds_cptr L = (lds_cptr)lds + lane * 16;
    bf16x8 wa[5], wb[5], oa[2], ob[2];
#define K3_LDF(wf, of, slot, k) do { const lds_cptr B_ = L + (slot) * K3_SLOT; of[0] = ldsr16(B_ + cw * 8192 + (k) * 1024); of[1] = ldsr16(B_ + cw * 8192 + 4096 + (k) * 1024); \
      _Pragma("unroll") for (int b = 0; b < 5; ++b) wf[b] = ldsr16(B_ + K3_AB + ((k) * 5 + b) * 1024); } while (0)
#define K3_MM(wf, of) do { _Pragma("unroll") for (int b = 0; b < 5; ++b) { acc[0][b] = MFMA16(wf[b], of[0], acc[0][b]); acc[1][b] = MFMA16(wf[b], of[1], acc[1][b]); } } while (0)
#define K3_IL() do { _Pragma("unroll") for (int i_ = 0; i_ < 7; ++i_) { __builtin_amdgcn_sched_group_barrier(0x008, 1, 0); __builtin_amdgcn_sched_group_barrier(0x100, 1, 0); } __builtin_amdgcn_sched_group_barrier(0x008, 3, 0); } while (0)
#define K3_STAGE3(slot) do { K3_LDF(wb, ob, slot, 1); K3_MM(wa, oa); K3_IL(); SBAR(); K3_LDF(wa, oa, slot, 2); K3_MM(wb, ob); K3_IL(); SBAR(); K3_LDF(wb, ob, slot, 3); K3_MM(wa, oa); K3_IL(); SBAR(); } while (0)
#define K3_NEXT(nslot) do { asm volatile("s_waitcnt lgkmcnt(0)" ::: "memory"); BARRIER(); SBAR(); K3_LDF(wa, oa, nslot, 0); K3_MM(wb, ob); K3_IL(); SBAR(); } while (0)
    TS(1);
    BARRIER();
    TS(2);
    K3_LDF(wa, oa, 0, 0); SBAR();
    K3_STAGE3(0); K3_NEXT(1);
    K3_STAGE3(1); K3_NEXT(2);
    K3_STAGE3(2); K3_NEXT(0);
    K3_STAGE3(0); K3_NEXT(1);
    K3_STAGE3(1);
    K3_MM(wb, ob);
    TS(3);
    AS3 char* S = (AS3 char*)lds + 2 * K3_SLOT;
    const int r = lane & 15, g = lane >> 4;
#pragma unroll
    for (int a = 0; a < 2; ++a)
#pragma unroll
      for (int b = 0; b < 5; ++b)
        *(AS3 u32x2*)(S + (cw * 32 + a * 16 + r) * 160 + b * 32 + g * 8) = u32x2{cvtpk(acc[a][b][0], acc[a][b][1]), cvtpk(acc[a][b][2], acc[a][b][3])};
    asm volatile("s_waitcnt lgkmcnt(0)" ::: "memory");
  }
  BARRIER();
  { const lds_cptr S = (lds_cptr)lds + 2 * K3_SLOT;
    char* ob = (char*)out + (size_t)R * 128 * 2560 + inst * 1280 + cg * 160;
#pragma unroll
    for (int i = 0; i < 3; ++i) { const int c = tid + 512 * i; if (c < 1280) { const int row = (c * 6554) >> 16, ch = c - row * 10; st16f<STF3>(ob + (unsigned)(row * 2560 + ch * 16), *(const AS3 u32x4*)(S + c * 16)); } }
  }
  TS(4);
  if (wid >= 4) TDUMP(2, ws, blockIdx.x * 8 + wid - 4);
}

extern "C" void kernel_launch(void* const* d_in, const int* in_sizes, int n_in, void* d_out, int out_size, void* d_ws, size_t ws_size, hipStream_t stream) {
  char* ws = (char*)d_ws;
  (void)hipFuncSetAttribute((const void*)k1_proj, hipFuncAttributeMaxDynamicSharedMemorySize, K1_LDS_ALL);
  (void)hipFuncSetAttribute((const void*)k2_attn, hipFuncAttributeMaxDynamicSharedMemorySize, ATT_LDS);
  (void)hipFuncSetAttribute((const void*)k3_out, hipFuncAttributeMaxDynamicSharedMemorySize, K3_LDS);
  k1_proj<<<256, 512, K1_LDS_ALL, stream>>>((const char*)d_in[0], (const char*)d_in[1], (const char*)d_in[3], (const char*)d_in[4], (const char*)d_in[5], (const char*)d_in[6],
                                            (const u16*)d_in[2], (const u16*)d_in[7], ws, (const char*)d_out);
  k2_attn<<<256, 576, ATT_LDS, stream>>>(ws, (const u16*)d_in[2], (const u16*)d_in[7]);
  k3_out<<<256, 512, K3_LDS, stream>>>(ws, (u16*)d_out);
}
```

```cpp
#include <hip/hip_runtime.h>
#include <hip/hip_bf16.h>
#include <stdint.h>

typedef short bf16x8 __attribute__((ext_vector_type(8)));
typedef short bf16x4 __attribute__((ext_vector_type(4)));
typedef float f32x16 __attribute__((ext_vector_type(16)));
typedef float f32x4 __attribute__((ext_vector_type(4)));
typedef float f32x2 __attribute__((ext_vector_type(2)));
typedef __bf16 bf16x2_t __attribute__((ext_vector_type(2)));
typedef unsigned u32x4 __attribute__((ext_vector_type(4)));
typedef unsigned u32x2 __attribute__((ext_vector_type(2)));
#define DI __device__ __forceinline__
#define AS3 __attribute__((address_space(3)))
typedef AS3 const char* lds_cptr;
typedef unsigned short u16;

constexpr int NH = 32;
constexpr int QTILE_B = 1280;
constexpr int KVTILE_B = 3072;
constexpr size_t Q_OFF = 0;
constexpr size_t Q_BYTES = (size_t)2 * NH * 64 * QTILE_B;
constexpr size_t KV_OFF = Q_OFF + Q_BYTES;
constexpr size_t KV_STREAM_B = (size_t)64 * KVTILE_B;
constexpr size_t KV_BYTES = 33 * KV_STREAM_B;
constexpr size_t O_OFF = KV_OFF + KV_BYTES;
constexpr size_t O_BYTES = (size_t)2 * 128 * 20 * 1024;
constexpr size_t WO_OFF = O_OFF + O_BYTES;
constexpr size_t WO_BYTES = (size_t)2 * 40 * 20 * 1024;
constexpr size_t FL_O = WO_OFF + WO_BYTES, FL_W = FL_O + 1024;
constexpr float QSCALE = 0.22360679774997896f * 1.4426950408889634f;

DI unsigned cvtpk(float lo, float hi) {
  f32x2 v = {lo, hi}; bf16x2_t b = __builtin_convertvector(v, bf16x2_t); return __builtin_bit_cast(unsigned, b);
}
#define MFMA32(a, b, c) __builtin_amdgcn_mfma_f32_32x32x16_bf16((a), (b), (c), 0, 0, 0)
#define MFMA32K8(a, b, c) __builtin_amdgcn_mfma_f32_32x32x8bf16_1k((a), (b), (c), 0, 0, 0)
#define MFMA16(a, b, c) __builtin_amdgcn_mfma_f32_16x16x32_bf16((a), (b), (c), 0, 0, 0)
#define SBAR() __builtin_amdgcn_sched_barrier(0)
#define WAITV(n) asm volatile("s_waitcnt vmcnt(" #n ")" ::: "memory")
#define BARRIER() __builtin_amdgcn_s_barrier()
DI bf16x8 ldsr16(lds_cptr p) { return *(const AS3 bf16x8*)p; }
DI bf16x4 ldsr8(lds_cptr p) { return *(const AS3 bf16x4*)p; }
typedef short v4i16_t __attribute__((ext_vector_type(4)));
DI bf16x4 ldstr(lds_cptr p) { return __builtin_bit_cast(bf16x4, __builtin_amdgcn_ds_read_tr16_b64_v4i16((AS3 v4i16_t*)p)); }
DI bf16x8 cat8(bf16x4 a, bf16x4 b) { return __builtin_shufflevector(a, b, 0, 1, 2, 3, 4, 5, 6, 7); }

DI void touch_lines(const char* sbase, unsigned voff, unsigned lds_dummy) {
  unsigned sv;
  asm volatile("s_mov_b32 %0, m0\n\ts_mov_b32 m0, %2\n\ts_nop 0\n\tglobal_load_lds_dword %3, %1\n\ts_mov_b32 m0, %0" : "=&s"(sv) : "s"(sbase), "s"(lds_dummy), "v"(voff) : "memory");
}
extern "C" __global__ void k_end();
constexpr int K1_XB = 16384, K1_WB = 20480, K1_SLOT = K1_XB + K1_WB;
constexpr int K1_KVB = 2560, K1_SLOTB = K1_SLOT + K1_KVB;
constexpr int K1_LDS_ALL = 4 * K1_SLOTB + 256;

template <bool ISK>
DI void store_qk_tile(char* ws, int inst, int n0, int row0, f32x4 acc, int lane) {
  const int g = lane >> 4, row = row0 + (lane & 15);
  const int G = (n0 >> 2) + g, head = (G * 52429) >> 18, dg = G - head * 5;
  const int tile = row >> 5, r32 = row & 31;
  const float sc = ISK ? 1.f : QSCALE;
  u32x2 v = {cvtpk(acc[0] * sc, acc[1] * sc), cvtpk(acc[2] * sc, acc[3] * sc)};
  const unsigned base = ISK ? (unsigned)KV_OFF + (unsigned)(inst ? 32 : head) * (unsigned)KV_STREAM_B + (unsigned)tile * KVTILE_B
                            : (unsigned)Q_OFF + ((unsigned)(inst * NH + head) * 64 + tile) * QTILE_B;
  const unsigned off = dg < 4 ? (dg >> 1) * 512 + r32 * 16 + (dg & 1) * 8 : 1024 + r32 * 8;
  *(u32x2*)(ws + (base + off)) = v;
}
DI void store_v_tile(char* ws, int inst, int n, int row0, f32x4 acc, int lane) {
  const int g = lane >> 4;
  const int head = (n * 52429) >> 20, d = n - head * 20;
  const int key = row0 + 4 * g, tile = key >> 5, k32 = key & 31;
  const int s = k32 >> 4, jj = (k32 >> 3) & 1, h = (k32 >> 2) & 1;
  u32x2 v = {cvtpk(acc[0], acc[1]), cvtpk(acc[2], acc[3])};
  const unsigned base = (unsigned)KV_OFF + (unsigned)(inst ? 32 : head) * (unsigned)KV_STREAM_B + (unsigned)tile * KVTILE_B;
  *(u32x2*)(ws + (base + 1536 + s * 768 + h * 384 + d * 16 + jj * 8)) = v;
}
DI void dma4(const char* sbase, unsigned ldsaddr, unsigned v0, unsigned v1, unsigned v2, unsigned v3) {
  unsigned sv;
  asm volatile("s_mov_b32 %0, m0\n\ts_mov_b32 m0, %2\n\ts_nop 0\n\t"
               "global_load_lds_dwordx4 %3, %1\n\tglobal_load_lds_dwordx4 %4, %1 offset:1024\n\tglobal_load_lds_dwordx4 %5, %1 offset:2048\n\tglobal_load_lds_dwordx4 %6, %1 offset:3072\n\t"
               "s_mov_b32 m0, %0" : "=&s"(sv) : "s"(sbase), "s"(ldsaddr), "v"(v0), "v"(v1), "v"(v2), "v"(v3) : "memory");
}
DI void dma1(const char* sbase, unsigned ldsaddr, unsigned v0) {
  unsigned sv;
  asm volatile("s_mov_b32 %0, m0\n\ts_mov_b32 m0, %2\n\ts_nop 0\n\tglobal_load_lds_dwordx4 %3, %1\n\ts_mov_b32 m0, %0" : "=&s"(sv) : "s"(sbase), "s"(ldsaddr), "v"(v0) : "memory");
}
template <bool ISB>
DI void k1_loader(char* lds, const char* x, const char* w, int ldw2, const char* wkv, int L, int lane, int st) {
  constexpr int NS = 4, SLOT = ISB ? K1_SLOTB : K1_SLOT;
  const unsigned lds0 = (unsigned)(uintptr_t)lds;
  unsigned vx[4], vw[5];
#pragma unroll
  for (int i = 0; i < 4; ++i) { const int row = 32 * L + 8 * i + (lane >> 3); vx[i] = (unsigned)(row * 1280 + (((lane & 7) ^ ((row >> 1) & 7)) << 4) - i * 1024); }
#pragma unroll
  for (int i = 0; i < 5; ++i) { const int c = (5 * L + i) * 64 + lane, k = (c * 52429) >> 20, cc = c - k * 20;
    vw[i] = (unsigned)(k * ldw2 + ((cc ^ (((k >> 3) & 1) << 1)) << 4) - (i & 3) * 1024 + 4096); }
  const char* wsrc = w - 4096;
  const char* kvsrc = wkv + L * 640;
  const unsigned ldx = lds0 + L * 4096, ldw_ = lds0 + K1_XB + L * 5120, ldkv = lds0 + K1_SLOT + L * 640, vo16 = lane * 16;
#define K1_STAGE(sq_, slot_) do { const int s_ = (sq_) + st >= 10 ? (sq_) + st - 10 : (sq_) + st; const char* xs_ = x + s_ * 128; const char* ws_ = wsrc + (size_t)s_ * 64 * ldw2; const unsigned o_ = (slot_) * SLOT; \
    dma4(xs_, ldx + o_, vx[0], vx[1], vx[2], vx[3]); dma4(ws_, ldw_ + o_, vw[0], vw[1], vw[2], vw[3]); dma1(ws_, ldw_ + o_ + 4096, vw[4]); \
    if constexpr (ISB) { if (lane < 40) dma1(kvsrc + s_ * 2560, ldkv + o_, vo16); } } while (0)
  K1_STAGE(0, 0);
  K1_STAGE(1, 1);
  if constexpr (ISB) WAITV(10); else WAITV(9);
  BARRIER();
  K1_STAGE(2, 2);
  K1_STAGE(3, 3);
  int slot = 0;
#pragma unroll 1
  for (int s = 0; s < 9; ++s) {
    if constexpr (ISB) { if (s < 7) WAITV(20); else if (s == 7) WAITV(10); else WAITV(0); }
    else { if (s < 7) WAITV(18); else if (s == 7) WAITV(9); else WAITV(0); }
    BARRIER();
    if (s + NS < 10) K1_STAGE(s + NS, slot);
    slot = slot == NS - 1 ? 0 : slot + 1;
  }
#undef K1_STAGE
}

template <int MODE>
DI void k1_body(char* lds, char* ws, int R, int cg, int wid, int lane) {
  constexpr int NS = 4, SLOT = MODE >= 2 ? K1_SLOTB : K1_SLOT;
  const int wr = wid >> 1, wc = wid & 1, r = lane & 15, g = lane >> 4;
  const lds_cptr L = (lds_cptr)lds;
  const int xb0 = (64 * wr + r) * 128 + ((g ^ (r >> 1)) << 4), xlo = xb0 + (MODE >= 2 ? wc * 4096 : 0), xhi = xb0 + (MODE >= 2 ? (wc ^ 1) * 4096 : 4096);
  const int q = (lane & 15) >> 2, p4 = lane & 3;
  const int wbase = K1_XB + (8 * g + q) * 320 + p4 * 8 + wc * 160;
  const int wbA = wbase + ((wc ^ g) & 1) * 32 - (wc & 1) * 32;
  const int wbB = wbase + (((wc ^ 1) ^ g) & 1) * 32 - ((wc ^ 1) & 1) * 32;
  const int kvb = K1_SLOT + (8 * g + q) * 40 + p4 * 8 + (cg & 1) * 32;
  f32x4 acc[4][5];
#pragma unroll
  for (int a = 0; a < 4; ++a)
#pragma unroll
    for (int b = 0; b < 5; ++b) acc[a][b] = f32x4{0, 0, 0, 0};
  f32x4 ex0 = {0, 0, 0, 0}, ex1 = {0, 0, 0, 0};
  bf16x8 xf0[4], wf0[5], xf1[4], wf1[5], kf0, kf1;
#define K1_LOAD(xf, wf, kf, B, ks) do { \
    _Pragma("unroll") for (int a = 0; a < 4; ++a) xf[a] = ldsr16((B) + (((a < 2 ? xlo : xhi)) ^ ((ks) * 64)) + (a & 1) * 2048); \
    _Pragma("unroll") for (int b = 0; b < 5; ++b) { const lds_cptr pa = (B) + ((b & 1) ? wbB : wbA) + b * 32 + (ks) * 10240; wf[b] = cat8(ldstr(pa), ldstr(pa + 1280)); } \
    if constexpr (MODE >= 2) { const lds_cptr pk = (B) + kvb + (ks) * 32 * 40; kf = cat8(ldstr(pk), ldstr(pk + 160)); } } while (0)
#define K1_MMX(xf, kf) do { if constexpr (MODE == 2) { ex0 = MFMA16(kf, xf[0], ex0); ex1 = MFMA16(kf, xf[1], ex1); } if constexpr (MODE == 3) { ex0 = MFMA16(xf[0], kf, ex0); ex1 = MFMA16(xf[1], kf, ex1); } } while (0)
#define K1_MMA(xf, wf, kf) do { \
    _Pragma("unroll") for (int a = 0; a < 4; ++a) _Pragma("unroll") for (int b = 0; b < 5; ++b) acc[a][b] = MODE == 1 ? MFMA16(xf[a], wf[b], acc[a][b]) : MFMA16(wf[b], xf[a], acc[a][b]); \
    K1_MMX(xf, kf); } while (0)
#define K1_SCHED() do { _Pragma("unroll") for (int i_ = 0; i_ < (MODE >= 2 ? 16 : 14); ++i_) { __builtin_amdgcn_sched_group_barrier(0x008, 1, 0); __builtin_amdgcn_sched_group_barrier(0x100, 1, 0); } \
    __builtin_amdgcn_sched_group_barrier(0x008, 8, 0); } while (0)
  const u32x4 ones = {0x3F803F80u, 0x3F803F80u, 0x3F803F80u, 0x3F803F80u};
  const u32x4 kone = {0x3F80u, 0u, 0x3F80u, 0u};
  if constexpr (MODE == 0) { if (cg >= 4) {
#pragma unroll
      for (int i = 0; i < 2; ++i) { const int t = wid * 64 + lane + 256 * i, head = (cg & 3) * 8 + (t >> 6), tile = R * 4 + ((t >> 4) & 3);
        *(u32x4*)(ws + (KV_OFF + (size_t)head * KV_STREAM_B + (size_t)tile * KVTILE_B + 1280 + (t & 15) * 16)) = kone; } } }
  if constexpr (MODE == 1) {
#pragma unroll
    for (int i = 0; i < 2; ++i) { const int t = wid * 64 + lane + 256 * i, head = (cg & 3) * 8 + (t >> 6), tile = R * 4 + ((t >> 4) & 3);
      *(u32x4*)(ws + (KV_OFF + (size_t)head * KV_STREAM_B + (size_t)tile * KVTILE_B + 1536 + ((t >> 2) & 3) * 384 + (20 + (t & 3)) * 16)) = (t & 3) ? u32x4{0, 0, 0, 0} : ones; }
  }
  BARRIER();
  K1_LOAD(xf0, wf0, kf0, L, 0);
  int slot = 0;
#pragma unroll 1
  for (int s = 0; s < 9; ++s) {
    const lds_cptr B = L + slot * SLOT;
    slot = slot == NS - 1 ? 0 : slot + 1;
    SBAR();
    K1_LOAD(xf1, wf1, kf1, B, 1);
    K1_MMA(xf0, wf0, kf0);
    K1_SCHED();
    SBAR();
    asm volatile("s_waitcnt lgkmcnt(0)" ::: "memory");
    BARRIER();
    const lds_cptr Bn = L + slot * SLOT;
    SBAR();
    K1_LOAD(xf0, wf0, kf0, Bn, 0);
    K1_MMA(xf1, wf1, kf1);
    K1_SCHED();
    SBAR();
  }
  { const lds_cptr B = L + slot * SLOT;
    K1_LOAD(xf1, wf1, kf1, B, 1);
    K1_MMA(xf0, wf0, kf0);
    K1_SCHED();
    SBAR(); }
  const int row0 = R * 128 + 64 * wr;
  const bool isK = MODE == 0 && cg >= 4;
  const unsigned hstride = (MODE == 1 || isK) ? (unsigned)KV_STREAM_B : 64u * QTILE_B, tstride = (MODE == 1 || isK) ? KVTILE_B : QTILE_B;
  const float sc = (MODE == 1 || isK) ? 1.f : QSCALE;
  char* base = ws + (MODE == 1 ? KV_OFF + 1536 : isK ? KV_OFF : Q_OFF + (MODE >= 2 ? (size_t)NH * 64 * QTILE_B : 0)) + (size_t)((cg & 3) * 8 + 4 * wc) * hstride + (size_t)(row0 >> 5) * tstride;
  auto store_col = [&](int b) {
    unsigned vo, astep;
    if constexpr (MODE == 1) {
      const int n = 16 * b + r, hl = (n * 52429) >> 20, d = n - hl * 20;
      vo = (g & 1) * 384 + (g >> 1) * 8 + hl * (unsigned)KV_STREAM_B + d * 16; astep = 768;
    } else {
      const int e = 4 * b + g, hl = (e * 13) >> 6, dg = e - 5 * hl;
      const unsigned rs = dg < 4 ? 16 : 8;
      vo = hl * hstride + (dg < 4 ? (dg >> 1) * 512 + (dg & 1) * 8 : 1024) + r * rs; astep = 16 * rs;
    }
#pragma unroll
    for (int a = 0; a < 4; ++a) { u32x2 v = {cvtpk(acc[a][b][0] * sc, acc[a][b][1] * sc), cvtpk(acc[a][b][2] * sc, acc[a][b][3] * sc)};
      *(u32x2*)(base + (vo + (MODE >= 2 ? (a >> 1) ^ wc : a >> 1) * tstride + (a & 1) * astep)) = v; }
  };
  K1_MMX(xf1, kf1);
#pragma unroll
  for (int b = 0; b < 5; ++b) {
#pragma unroll
    for (int a = 0; a < 4; ++a) acc[a][b] = MODE == 1 ? MFMA16(xf1[a], wf1[b], acc[a][b]) : MFMA16(wf1[b], xf1[a], acc[a][b]);
    SBAR();
    if (b > 0) { store_col(b - 1); SBAR(); }
  }
  store_col(4);
  if constexpr (MODE >= 2) {
    const int er = row0 + 32 * wc;
    char* tb = ws + KV_OFF + 32 * KV_STREAM_B + (size_t)(er >> 5) * KVTILE_B;
    if constexpr (MODE == 2) {
      if (!(cg & 1) || g == 0) { store_qk_tile<true>(ws, 1, 16 * (cg & 1), er, ex0, lane); store_qk_tile<true>(ws, 1, 16 * (cg & 1), er + 16, ex1, lane); }
      if (cg == 0 && lane < 16) *(u32x4*)(tb + 1280 + lane * 16) = kone;
    } else {
      if (!(cg & 1) || r < 4) { store_v_tile(ws, 1, 16 * (cg & 1) + r, er, ex0, lane); store_v_tile(ws, 1, 16 * (cg & 1) + r, er + 16, ex1, lane); }
      if (cg == 2 && lane < 16) *(u32x4*)(tb + 1536 + (lane >> 2) * 384 + (20 + (lane & 3)) * 16) = (lane & 3) ? u32x4{0, 0, 0, 0} : ones;
    }
  }
}

extern "C" __global__ __launch_bounds__(512, 2) void k1_proj(const char* xA, const char* wqkv, const char* xB, const char* wq, const char* wk, const char* wv, char* ws) {
  extern __shared__ __attribute__((aligned(16))) char lds[];
  const int lane = threadIdx.x & 63, wid = __builtin_amdgcn_readfirstlane(threadIdx.x >> 6);
  const int xcd = blockIdx.x & 7, j = blockIdx.x >> 3;
  const bool isA = xcd < 6;
  const int R = (isA ? (xcd / 3) * 8 : (xcd - 6) * 8) + (j & 7), cg = (isA ? (xcd % 3) * 4 : 0) + (j >> 3);
  const int st = (3 * j) % 10;
  if (wid < 4) {
    if (isA) k1_loader<false>(lds, xA + (size_t)R * 128 * 1280, wqkv + cg * 320, 3840, nullptr, wid, lane, st);
    else k1_loader<true>(lds, xB + (size_t)R * 128 * 1280, wq + cg * 320, 1280, cg >= 2 ? wv : wk, wid, lane, st);
  } else {
    if (blockIdx.x == 0 && wid == 5) { *(u32x4*)(ws + FL_O + lane * 16) = u32x4{0, 0, 0, 0}; *(u32x4*)(ws + FL_W + lane * 16) = u32x4{0, 0, 0, 0}; }
    const unsigned dummy = (unsigned)(uintptr_t)lds + K1_LDS_ALL - 256;
    if (wid == 4 && j < 5) {
      unsigned long long pc; unsigned off, len;
      asm volatile("s_getpc_b64 %0\n1:\n\ts_mov_b32 %1, 1b-k1_proj\n\ts_mov_b32 %2, k_end-k1_proj" : "=s"(pc), "=s"(off), "=s"(len));
      const unsigned vo = j * 8192 + lane * 128; if (vo < len) touch_lines((const char*)(pc - off), vo, dummy);
    }
    if (isA) { if (cg < 8) k1_body<0>(lds, ws, R, cg, wid - 4, lane); else k1_body<1>(lds, ws, R, cg, wid - 4, lane); }
    else { if (cg >= 2) k1_body<3>(lds, ws, R, cg, wid - 4, lane); else k1_body<2>(lds, ws, R, cg, wid - 4, lane); }
  }
}

DI void dma4c(const char* sbase, unsigned ldsaddr, unsigned v0) {
  unsigned sv;
  asm volatile("s_mov_b32 %0, m0\n\ts_mov_b32 m0, %2\n\ts_nop 0\n\t"
               "global_load_lds_dwordx4 %3, %1 sc1\n\tglobal_load_lds_dwordx4 %3, %1 offset:1024 sc1\n\tglobal_load_lds_dwordx4 %3, %1 offset:2048 sc1\n\tglobal_load_lds_dwordx4 %3, %1 offset:3072 sc1\n\t"
               "s_mov_b32 m0, %0" : "=&s"(sv) : "s"(sbase), "s"(ldsaddr), "v"(v0) : "memory");
}
DI void dma1c(const char* sbase, unsigned ldsaddr, unsigned v0) {
  unsigned sv;
  asm volatile("s_mov_b32 %0, m0\n\ts_mov_b32 m0, %2\n\ts_nop 0\n\tglobal_load_lds_dwordx4 %3, %1 sc1\n\ts_mov_b32 m0, %0" : "=&s"(sv) : "s"(sbase), "s"(ldsaddr), "v"(v0) : "memory");
}
DI void st16_wt(char* sbase, unsigned voff, u32x4 v) { asm volatile("global_store_dwordx4 %0, %1, %2 sc1\n\ts_nop 1" :: "v"(voff), "v"(v), "s"(sbase) : "memory"); }
DI void st8_wt(char* sbase, unsigned voff, u32x2 v) { asm volatile("global_store_dwordx2 %0, %1, %2 sc1" :: "v"(voff), "v"(v), "s"(sbase) : "memory"); }
typedef __attribute__((address_space(1))) unsigned gu32;
DI unsigned ld_flag(const char* p) { return __hip_atomic_load((gu32*)p, __ATOMIC_RELAXED, __HIP_MEMORY_SCOPE_AGENT); }
DI void st_flag(char* p, unsigned v) { __hip_atomic_store((gu32*)p, v, __ATOMIC_RELAXED, __HIP_MEMORY_SCOPE_AGENT); }
constexpr int K3_AB = 32768, K3_BB = 20480, K3_SLOT = K3_AB + K3_BB;
DI void out_phase(char* lds, const char* ws, u16* out, int inst, int R, int cg, int wid, int lane) {
  const int tid = wid * 64 + lane;
  const unsigned lds0 = (unsigned)(uintptr_t)lds, vo16 = lane * 16;
  if (wid < 4) {
    const int L = wid;
    const char* wsrc = ws + WO_OFF + (size_t)(inst * 8 + cg) * 100 * 1024 + L * 5120;
    const char* osrc = ws + O_OFF + (size_t)(inst * 128 + R * 8 + 2 * L) * 20 * 1024;
#define K3_LDA(G, slot) do { const unsigned o_ = lds0 + (slot) * K3_SLOT; dma4c(osrc + (G) * 4096, o_ + L * 8192, vo16); dma4c(osrc + 20480 + (G) * 4096, o_ + L * 8192 + 4096, vo16); } while (0)
#define K3_LDB(G, slot) do { const unsigned o_ = lds0 + (slot) * K3_SLOT; dma4c(wsrc + (G) * 20480, o_ + K3_AB + L * 5120, vo16); dma1c(wsrc + (G) * 20480 + 4096, o_ + K3_AB + L * 5120 + 4096, vo16); } while (0)
    K3_LDB(0, 0); K3_LDB(1, 1); K3_LDB(2, 2);
    BARRIER();
    K3_LDA(0, 0); K3_LDA(1, 1); WAITV(8); BARRIER();
    K3_LDA(2, 2); WAITV(8); BARRIER();
    K3_LDA(3, 0); K3_LDB(3, 0); WAITV(13); BARRIER();
    K3_LDA(4, 1); K3_LDB(4, 1); WAITV(13); BARRIER();
    WAITV(0); BARRIER();
  } else {
    const int cw = wid - 4;
    f32x4 acc[2][5];
#pragma unroll
    for (int a = 0; a < 2; ++a)
#pragma unroll
      for (int b = 0; b < 5; ++b) acc[a][b] = f32x4{0, 0, 0, 0};
    const lds_cptr L = (lds_cptr)lds + lane * 16;
    bf16x8 wa[5], wb[5], oa[2], ob[2];
#define K3_LDF(wf, of, slot, k) do { const lds_cptr B_ = L + (slot) * K3_SLOT; of[0] = ldsr16(B_ + cw * 8192 + (k) * 1024); of[1] = ldsr16(B_ + cw * 8192 + 4096 + (k) * 1024); \
      _Pragma("unroll") for (int b = 0; b < 5; ++b) wf[b] = ldsr16(B_ + K3_AB + ((k) * 5 + b) * 1024); } while (0)
#define K3_MM(wf, of) do { _Pragma("unroll") for (int b = 0; b < 5; ++b) { acc[0][b] = MFMA16(wf[b], of[0], acc[0][b]); acc[1][b] = MFMA16(wf[b], of[1], acc[1][b]); } } while (0)
#define K3_IL() do { _Pragma("unroll") for (int i_ = 0; i_ < 7; ++i_) { __builtin_amdgcn_sched_group_barrier(0x008, 1, 0); __builtin_amdgcn_sched_group_barrier(0x100, 1, 0); } __builtin_amdgcn_sched_group_barrier(0x008, 3, 0); } while (0)
#define K3_STAGE3(slot) do { K3_LDF(wb, ob, slot, 1); K3_MM(wa, oa); K3_IL(); SBAR(); K3_LDF(wa, oa, slot, 2); K3_MM(wb, ob); K3_IL(); SBAR(); K3_LDF(wb, ob, slot, 3); K3_MM(wa, oa); K3_IL(); SBAR(); } while (0)
#define K3_NEXT(nslot) do { asm volatile("s_waitcnt lgkmcnt(0)" ::: "memory"); BARRIER(); SBAR(); K3_LDF(wa, oa, nslot, 0); K3_MM(wb, ob); K3_IL(); SBAR(); } while (0)
    BARRIER();
    BARRIER();
    K3_LDF(wa, oa, 0, 0); SBAR();
    K3_STAGE3(0); K3_NEXT(1);
    K3_STAGE3(1); K3_NEXT(2);
    K3_STAGE3(2); K3_NEXT(0);
    K3_STAGE3(0); K3_NEXT(1);
    K3_STAGE3(1);
    K3_MM(wb, ob);
    AS3 char* S = (AS3 char*)lds + 2 * K3_SLOT;
    const int r = lane & 15, g = lane >> 4;
#pragma unroll
    for (int a = 0; a < 2; ++a)
#pragma unroll
      for (int b = 0; b < 5; ++b)
        *(AS3 u32x2*)(S + (cw * 32 + a * 16 + r) * 160 + b * 32 + g * 8) = u32x2{cvtpk(acc[a][b][0], acc[a][b][1]), cvtpk(acc[a][b][2], acc[a][b][3])};
    asm volatile("s_waitcnt lgkmcnt(0)" ::: "memory");
  }
  BARRIER();
  { const lds_cptr S = (lds_cptr)lds + 2 * K3_SLOT;
    char* ob = (char*)out + (size_t)R * 128 * 2560 + inst * 1280 + cg * 160;
#pragma unroll
    for (int i = 0; i < 3; ++i) { const int c = tid + 512 * i; if (c < 1280) { const int row = (c * 6554) >> 16, ch = c - row * 10; *(u32x4*)(ob + (unsigned)(row * 2560 + ch * 16)) = *(const AS3 u32x4*)(S + c * 16); } }
  }
}

constexpr int NSLOT = 6, CHUNK_B = 8 * KVTILE_B;
constexpr int K23_DUMMY = 3 * 53248, ATT_LDS = K23_DUMMY + 256;
DI void dma_tile(const char* gsrc, unsigned voff, unsigned lds_dst) {
  unsigned sv;
  asm volatile("s_mov_b32 %0, m0\n\ts_mov_b32 m0, %3\n\ts_nop 0\n\tglobal_load_lds_dwordx4 %2, %1\n\tglobal_load_lds_dwordx4 %2, %1 offset:1024\n\tglobal_load_lds_dwordx4 %2, %1 offset:2048\n\ts_mov_b32 m0, %0"
               : "=&s"(sv) : "s"(gsrc), "v"(voff), "s"(lds_dst) : "memory");
}
DI float ex2(float x) { return __builtin_amdgcn_exp2f(x); }
DI float swap_max(float m) { auto rr = __builtin_amdgcn_permlane32_swap(__float_as_uint(m), __float_as_uint(m), false, false); return fmaxf(__uint_as_float(rr[0]), __uint_as_float(rr[1])); }
DI float max16(const f32x16& s) { float a = fmaxf(s[0], s[1]);
#pragma unroll
  for (int i = 2; i < 16; ++i) a = fmaxf(a, s[i]); return a; }
DI f32x16 splat16(float v) { f32x16 r;
#pragma unroll
  for (int i = 0; i < 16; ++i) r[i] = v; return r; }
#define BC8(x) __builtin_bit_cast(bf16x8, x)

extern "C" __global__ __launch_bounds__(576, 3) void k2_attn(char* ws, const u16* woA, const u16* woB, u16* out) {
  extern __shared__ __attribute__((aligned(16))) char lds[];
  const int tid = threadIdx.x, lane = tid & 63, r32 = lane & 31, hi = lane >> 5;
  const int wid = __builtin_amdgcn_readfirstlane(tid >> 6);
  const int xcd = blockIdx.x & 7, j = blockIdx.x >> 3;
  const int inst = j >> 4, head = 4 * xcd + ((j & 15) >> 2), qb = j & 3;
  const char* kvs = ws + KV_OFF + (size_t)(inst ? 32 : head) * KV_STREAM_B;
  const char* gsrc = kvs + (size_t)wid * KVTILE_B;
  const unsigned lds0 = (unsigned)(uintptr_t)lds, ldst = lds0 + wid * KVTILE_B, vo16 = lane * 16;
  if (wid == 8) {
    __builtin_amdgcn_s_setprio(3);
    { unsigned long long pc; unsigned off, len;
      asm volatile("s_getpc_b64 %0\n1:\n\ts_mov_b32 %1, 1b-k2_attn\n\ts_mov_b32 %2, k_end-k2_attn" : "=s"(pc), "=s"(off), "=s"(len));
      if (j < 3 && j * 8192 + lane * 128u < len) touch_lines((const char*)(pc - off), j * 8192 + lane * 128, lds0 + K23_DUMMY); }
#define K2_CHUNK(c, slot_) do { _Pragma("unroll") for (int i_ = 0; i_ < 6; ++i_) dma4(kvs + (size_t)(c) * CHUNK_B + i_ * 4096, lds0 + (slot_) * CHUNK_B + i_ * 4096, vo16, vo16, vo16, vo16); } while (0)
    BARRIER();
    K2_CHUNK(1, 1); K2_CHUNK(2, 2); WAITV(24); K2_CHUNK(3, 3); WAITV(24); K2_CHUNK(4, 4); WAITV(24);
    BARRIER();
    K2_CHUNK(5, 5); WAITV(0);
    BARRIER();
    K2_CHUNK(6, 0); K2_CHUNK(7, 1); WAITV(0);
    BARRIER();
    bf16x8 wv_[7];
#pragma unroll
    for (int i = 0; i < 7; ++i) {
      const int f = min((int)blockIdx.x + 256 * i, 1599), inst_ = f >= 800, ff = f - 800 * inst_, cgp = ff / 100, rem = ff - cgp * 100, ks = rem / 5, nt = rem - ks * 5;
      const u16* src = (inst_ ? woB : woA) + (size_t)(ks * 32 + (lane >> 4) * 8) * 640 + cgp * 80 + nt * 16 + (lane & 15);
#pragma unroll
      for (int e = 0; e < 8; ++e) wv_[i][e] = (short)src[e * 640];
    }
#pragma unroll
    for (int i = 0; i < 7; ++i) if ((int)blockIdx.x + 256 * i < 1600) st16_wt(ws + WO_OFF + (size_t)(blockIdx.x + 256 * i) * 1024, lane * 16, __builtin_bit_cast(u32x4, wv_[i]));
    asm volatile("s_waitcnt vmcnt(0)" ::: "memory");
    if (lane == 0) st_flag(ws + FL_W + blockIdx.x * 4, 1u);
    return;
  }
  dma_tile(gsrc, vo16, ldst);
  const int qt0 = qb * 16 + wid * 2;
  const char* qsrc = ws + Q_OFF + ((size_t)(inst * NH + head) * 64 + qt0) * QTILE_B;
  bf16x8 QA1, QB1; bf16x4 QA2, QB2;
  { const unsigned vo8 = r32 * 8;
    asm volatile("global_load_dwordx4 %0, %4, %6\n\tglobal_load_dwordx2 %1, %5, %6 offset:1024\n\tglobal_load_dwordx4 %2, %4, %6 offset:1280\n\tglobal_load_dwordx2 %3, %5, %6 offset:2304"
                 : "=&v"(QA1), "=&v"(QA2), "=&v"(QB1), "=&v"(QB2) : "v"(vo16), "v"(vo8), "s"(qsrc) : "memory"); }
  const lds_cptr kp1 = (lds_cptr)lds + lane * 16;
  const lds_cptr kp2 = (lds_cptr)lds + 1024 + lane * 8;
  const lds_cptr vp = (lds_cptr)lds + 1536 + hi * 384 + r32 * 16;
  f32x16 OA = splat16(0.f), OB = splat16(0.f);
  u32x4 PA0, PA1, PB0, PB1 = {0, 0, 0, 0};
  bf16x8 V0 = {0, 0, 0, 0, 0, 0, 0, 0}, V1 = {0, 0, 0, 0, 0, 0, 0, 0};
  asm volatile("s_waitcnt vmcnt(0)" : "+v"(QA1), "+v"(QA2), "+v"(QB1), "+v"(QB2) :: "memory");
  if (hi) { QA2 = bf16x4{0, 0, 0, 0}; QB2 = bf16x4{0, 0, 0, 0}; }
  BARRIER();
  bf16x8 K1f = ldsr16(kp1); bf16x4 K2f = ldsr8(kp2);
  const f32x16 z16 = splat16(0.f);
  { const f32x16 a = MFMA32K8(K2f, QA2, MFMA32(K1f, QA1, z16)), b = MFMA32K8(K2f, QB2, MFMA32(K1f, QB1, z16));
    const unsigned na = __float_as_uint(-swap_max(max16(a))), nb = __float_as_uint(-swap_max(max16(b)));
    const unsigned ua = (na + ((na >> 31) ? 0u : 0xFFFFu)) >> 16, ub = (nb + ((nb >> 31) ? 0u : 0xFFFFu)) >> 16;
    if (hi) { QA2 = __builtin_bit_cast(bf16x4, u32x2{ua, 0u}); QB2 = __builtin_bit_cast(bf16x4, u32x2{ub, 0u}); } }
  f32x16 SA0 = MFMA32K8(K2f, QA2, MFMA32(K1f, QA1, z16)), SB0 = MFMA32K8(K2f, QB2, MFMA32(K1f, QB1, z16)), SA1, SB1;
  K1f = ldsr16(kp1 + KVTILE_B); K2f = ldsr8(kp2 + KVTILE_B);
#define GRP(S, i, P, k) do { SBAR(); S[i] = ex2(S[i]); S[i + 1] = ex2(S[i + 1]); S[i + 2] = ex2(S[i + 2]); S[i + 3] = ex2(S[i + 3]); \
      P[k] = cvtpk(S[i], S[i + 1]); P[k + 1] = cvtpk(S[i + 2], S[i + 3]); SBAR(); } while (0)
#define STEP(SA, SB, SAn, SBn, voff, k1n, k2n) do { \
      V0 = ldsr16(voff);                 SAn = MFMA32(K1f, QA1, z16);   GRP(SA, 0, PA0, 0); \
                                         SAn = MFMA32K8(K2f, QA2, SAn); GRP(SA, 4, PA0, 2); \
                                         OB = MFMA32(V1, BC8(PB1), OB); GRP(SA, 8, PA1, 0); \
      V1 = ldsr16((voff) + 768);         OA = MFMA32(V0, BC8(PA0), OA); GRP(SA, 12, PA1, 2); \
                                         SBn = MFMA32(K1f, QB1, z16);   GRP(SB, 0, PB0, 0); \
      K1f = ldsr16(k1n);                 SBn = MFMA32K8(K2f, QB2, SBn); GRP(SB, 4, PB0, 2); \
      K2f = ldsr8(k2n);                  OA = MFMA32(V1, BC8(PA1), OA); GRP(SB, 8, PB1, 0); \
                                         OB = MFMA32(V0, BC8(PB0), OB); GRP(SB, 12, PB1, 2); \
    } while (0)
#define PRIO(n) __builtin_amdgcn_s_setprio(n)
  int slot = 0;
#pragma unroll 1
  for (int c = 0; c < 8; ++c) {
    const int so = slot * CHUNK_B;
    const int nslot = slot == NSLOT - 1 ? 0 : slot + 1;
    const int sn = nslot * CHUNK_B;
    const lds_cptr k1c = kp1 + so, k2c = kp2 + so, vc = vp + so, k1x = kp1 + sn, k2x = kp2 + sn;
    PRIO(2);
    STEP(SA0, SB0, SA1, SB1, vc + 0 * KVTILE_B, k1c + 2 * KVTILE_B, k2c + 2 * KVTILE_B);
    STEP(SA1, SB1, SA0, SB0, vc + 1 * KVTILE_B, k1c + 3 * KVTILE_B, k2c + 3 * KVTILE_B);
    PRIO(1);
    STEP(SA0, SB0, SA1, SB1, vc + 2 * KVTILE_B, k1c + 4 * KVTILE_B, k2c + 4 * KVTILE_B);
    STEP(SA1, SB1, SA0, SB0, vc + 3 * KVTILE_B, k1c + 5 * KVTILE_B, k2c + 5 * KVTILE_B);
    PRIO(0);
    STEP(SA0, SB0, SA1, SB1, vc + 4 * KVTILE_B, k1c + 6 * KVTILE_B, k2c + 6 * KVTILE_B);
    STEP(SA1, SB1, SA0, SB0, vc + 5 * KVTILE_B, k1c + 7 * KVTILE_B, k2c + 7 * KVTILE_B);
    if (c == 0 || c == 3 || c == 5) BARRIER();
    SBAR();
    PRIO(3);
    STEP(SA0, SB0, SA1, SB1, vc + 6 * KVTILE_B, k1x, k2x);
    STEP(SA1, SB1, SA0, SB0, vc + 7 * KVTILE_B, k1x + KVTILE_B, k2x + KVTILE_B);
    slot = nslot;
  }
  OB = MFMA32(V1, BC8(PB1), OB);
  float lA, lB;
  { auto rr = __builtin_amdgcn_permlane32_swap(__float_as_uint(OA[8]), __float_as_uint(OA[8]), false, false); lA = __uint_as_float(rr[1]);
    auto rs = __builtin_amdgcn_permlane32_swap(__float_as_uint(OB[8]), __float_as_uint(OB[8]), false, false); lB = __uint_as_float(rs[1]); }
  float chk = 0.f;
#pragma unroll
  for (int i = 0; i < 9; ++i) { chk = fmaf(OA[i], 0.f, chk); chk = fmaf(OB[i], 0.f, chk); }
  if (!hi) {
#pragma unroll
    for (int i = 9; i < 12; ++i) { chk = fmaf(OA[i], 0.f, chk); chk = fmaf(OB[i], 0.f, chk); } }
  const bool bad = !(chk == 0.f) || !(lA < 1.8e19f) || !(lB < 1.8e19f);
  if (__builtin_expect(__any(bad), 0)) {
    const f32x16 z = splat16(0.f);
    if (hi) { QA2 = bf16x4{0, 0, 0, 0}; QB2 = bf16x4{0, 0, 0, 0}; }
    float mA = -INFINITY, mB = -INFINITY;
    for (int t = 0; t < 64; ++t) {
      const bf16x8 k1 = *(const bf16x8*)(kvs + (size_t)t * KVTILE_B + lane * 16);
      const bf16x4 k2 = *(const bf16x4*)(kvs + (size_t)t * KVTILE_B + 1024 + r32 * 8);
      mA = fmaxf(mA, max16(MFMA32K8(k2, QA2, MFMA32(k1, QA1, z))));
      mB = fmaxf(mB, max16(MFMA32K8(k2, QB2, MFMA32(k1, QB1, z))));
    }
    const f32x16 negA = splat16(-swap_max(mA)), negB = splat16(-swap_max(mB));
    OA = z; OB = z;
    for (int t = 0; t < 64; ++t) {
      const char* tb = kvs + (size_t)t * KVTILE_B;
      const bf16x8 k1 = *(const bf16x8*)(tb + lane * 16);
      const bf16x4 k2 = *(const bf16x4*)(tb + 1024 + r32 * 8);
      const bf16x8 v0 = *(const bf16x8*)(tb + 1536 + hi * 384 + r32 * 16), v1 = *(const bf16x8*)(tb + 1536 + 768 + hi * 384 + r32 * 16);
      f32x16 a = MFMA32K8(k2, QA2, MFMA32(k1, QA1, negA)), b = MFMA32K8(k2, QB2, MFMA32(k1, QB1, negB));
      u32x4 pa0, pa1, pb0, pb1;
#pragma unroll
      for (int i = 0; i < 4; ++i) { pa0[i] = cvtpk(ex2(a[2 * i]), ex2(a[2 * i + 1])); pa1[i] = cvtpk(ex2(a[8 + 2 * i]), ex2(a[9 + 2 * i]));
                                    pb0[i] = cvtpk(ex2(b[2 * i]), ex2(b[2 * i + 1])); pb1[i] = cvtpk(ex2(b[8 + 2 * i]), ex2(b[9 + 2 * i])); }
      OA = MFMA32(v1, BC8(pa1), MFMA32(v0, BC8(pa0), OA)); OB = MFMA32(v1, BC8(pb1), MFMA32(v0, BC8(pb0), OB));
    }
    auto rr = __builtin_amdgcn_permlane32_swap(__float_as_uint(OA[8]), __float_as_uint(OA[8]), false, false); lA = __uint_as_float(rr[1]);
    auto rs = __builtin_amdgcn_permlane32_swap(__float_as_uint(OB[8]), __float_as_uint(OB[8]), false, false); lB = __uint_as_float(rs[1]);
  }
  char* obase = ws + O_OFF + (size_t)inst * 128 * 20 * 1024 + (size_t)qt0 * 2 * 20 * 1024;
  const unsigned rowoff = (r32 >> 4) * 20 * 1024 + (r32 & 15) * 16, G0 = 5 * head;
#pragma unroll
  for (int q = 0; q < 2; ++q) {
    const f32x16& O = q ? OB : OA;
    const float inv = __builtin_amdgcn_rcpf(q ? lB : lA);
    unsigned w[6];
#pragma unroll
    for (int u = 0; u < 3; ++u) { w[2 * u] = cvtpk(O[4 * u] * inv, O[4 * u + 1] * inv); w[2 * u + 1] = cvtpk(O[4 * u + 2] * inv, O[4 * u + 3] * inv); }
    u32x4 c; u32x2 h; unsigned Gc, Gh;
    if (!(head & 1)) {
      const auto s0 = __builtin_amdgcn_permlane32_swap(w[0], w[2], false, false), s1 = __builtin_amdgcn_permlane32_swap(w[1], w[3], false, false);
      c = u32x4{s0[0], s1[0], s0[1], s1[1]}; Gc = G0 + 2 * hi; h = u32x2{w[4], w[5]}; Gh = G0 + 4;
    } else {
      const auto t0 = __builtin_amdgcn_permlane32_swap(w[0], w[4], false, false), t1 = __builtin_amdgcn_permlane32_swap(w[1], w[5], false, false);
      const unsigned o0 = hi ? t0[0] : t0[1], o1 = hi ? t1[0] : t1[1];
      c = hi ? u32x4{w[2], w[3], o0, o1} : u32x4{o0, o1, w[2], w[3]}; Gc = G0 + 1 + 2 * hi; h = u32x2{w[0], w[1]}; Gh = G0;
    }
    st16_wt(obase, rowoff + q * 2 * 20 * 1024 + (Gc >> 3) * 1024 + ((Gc >> 1) & 3) * 256, c);
    if (!hi) st8_wt(obase, rowoff + q * 2 * 20 * 1024 + (Gh >> 3) * 1024 + ((Gh >> 1) & 3) * 256 + (Gh & 1) * 8, h);
  }
  const int inst3 = xcd >> 2, R3 = (xcd & 3) * 4 + (j & 3), cg3 = j >> 2;
  if (wid < 4) {
    for (unsigned spins = 0; spins < (1u << 16); ++spins) {
      const unsigned f = ld_flag(ws + FL_W + (wid * 64 + lane) * 4);
      const bool ok = __all(f == 1u);
      if (ok) break;
      __builtin_amdgcn_s_sleep(2);
    }
  }
  asm volatile("s_waitcnt vmcnt(0) lgkmcnt(0)" ::: "memory");
  BARRIER();
  if (wid == 4) {
    if (lane == 0) st_flag(ws + FL_O + ((inst * 4 + qb) * 32 + head) * 4, 1u);
    for (unsigned spins = 0; spins < (1u << 16); ++spins) {
      if (__all(ld_flag(ws + FL_O + (xcd * 32 + (lane & 31)) * 4) == 1u)) break;
      __builtin_amdgcn_s_sleep(1);
    }
  }
  __builtin_amdgcn_fence(__ATOMIC_ACQUIRE, "wavefront");
  out_phase(lds, ws, out, inst3, R3, cg3, wid, lane);
}
extern "C" __global__ void k_end() {}

extern "C" void kernel_launch(void* const* d_in, const int* in_sizes, int n_in, void* d_out, int out_size, void* d_ws, size_t ws_size, hipStream_t stream) {
  char* ws = (char*)d_ws;
  (void)hipFuncSetAttribute((const void*)k1_proj, hipFuncAttributeMaxDynamicSharedMemorySize, K1_LDS_ALL);
  (void)hipFuncSetAttribute((const void*)k2_attn, hipFuncAttributeMaxDynamicSharedMemorySize, ATT_LDS);
  k1_proj<<<256, 512, K1_LDS_ALL, stream>>>((const char*)d_in[0], (const char*)d_in[1], (const char*)d_in[3], (const char*)d_in[4], (const char*)d_in[5], (const char*)d_in[6], ws);
  k2_attn<<<256, 576, ATT_LDS, stream>>>(ws, (const u16*)d_in[2], (const u16*)d_in[7], (u16*)d_out);
}
```
